```python
import torch

def _softmax(x, axis=-1):
    return torch.softmax(x, dim=axis)
def _gelu(x):
    return torch.nn.functional.gelu(x, approximate="tanh")
def _silu(x):
    return torch.nn.functional.silu(x)
def _sigmoid(x):
    return torch.sigmoid(x)
def _layernorm(x, axis=-1, eps=1e-5):
    m = x.mean(dim=axis, keepdim=True)
    v = x.var(dim=axis, keepdim=True, unbiased=False)
    return (x - m) / torch.sqrt(v + eps)
def _scatter_add(tgt, idx, src, axis=0):
    return tgt.clone().index_add_(axis, idx, src)
def _segment_sum(src, seg, n):
    out = torch.zeros((n,) + src.shape[1:], dtype=src.dtype)
    return out.index_add_(0, seg, src)
def _pad(x, pw):
    flat = tuple(v for pair in reversed(pw) for v in pair)
    return torch.nn.functional.pad(x, flat)

kernel_name = 'gated_mix__ssm_conv_h3__geglu_ffn__prompt'

def setup_inputs():

    _w = (lambda a: (lambda t: t.to(torch.bfloat16) if t.is_floating_point() else t)(torch.as_tensor(a)))
    def _A_setup():
        B, S, D = 1, 2048, 512
        ks = 4
        return {
            "q": _w((lambda *s: torch.randn(*s, dtype=torch.bfloat16) * 0.05)(B, S, D)),
            "k": _w((lambda *s: torch.randn(*s, dtype=torch.bfloat16) * 0.2)(B, S, D)),
            "v": _w((lambda *s: torch.randn(*s, dtype=torch.bfloat16) * 0.2)(B, S, D)),
            "shift_k": _w((lambda *s: torch.randn(*s, dtype=torch.bfloat16) * 0.2)(D, ks) * 0.5),
            "ssm_k": _w((lambda *s: torch.randn(*s, dtype=torch.bfloat16) * 0.8)(D, S) * (1.0 / S ** 0.5)),
            "ssm_D": _w((lambda *s: torch.randn(*s, dtype=torch.bfloat16) * 0.2)(D)),
        }
    _A_in = _A_setup()

    def _B_setup():
        B, S, D, F = 1, 2048, 512, 1376
        return {
            "x": _w((lambda *s: torch.randn(*s, dtype=torch.bfloat16) * 0.05)(B, S, D)),
            "w_gate": _w((lambda *s: torch.randn(*s, dtype=torch.bfloat16) * 0.05)(D, F)),
            "w_up": _w((lambda *s: torch.randn(*s, dtype=torch.bfloat16) * 0.05)(D, F)),
            "w_down": _w((lambda *s: torch.randn(*s, dtype=torch.bfloat16) * 0.05)(F, D)),
        }
    _B_in = _B_setup()

    return {
        "A_q": _A_in["q"],
        "A_k": _A_in["k"],
        "A_v": _A_in["v"],
        "A_shift_k": _A_in["shift_k"],
        "A_ssm_k": _A_in["ssm_k"],
        "A_ssm_D": _A_in["ssm_D"],
        "B_x": _B_in["x"],
        "B_w_gate": _B_in["w_gate"],
        "B_w_up": _B_in["w_up"],
        "B_w_down": _B_in["w_down"],
        "gate_w": _w((lambda *s: torch.randn(*s, dtype=torch.bfloat16) * 0.05)(512) * 16.0),
    }

def reference(A_q, A_k, A_v, A_shift_k, A_ssm_k, A_ssm_D, B_x, B_w_gate, B_w_up, B_w_down, gate_w):
    def _A_ref(q, k, v, shift_k, ssm_k, ssm_D):
        B, S, D = q.shape
        ks = shift_k.shape[-1]
        k_pad = _pad(k, ((0, 0), (ks - 1, 0), (0, 0)))
        k_shift = torch.zeros_like(k)
        for t in range(ks):
            k_shift = k_shift + k_pad[:, t:t + S, :] * shift_k[:, ks - 1 - t][None, None, :]
        kv = k_shift * v
        L = 2 * S
        kv_pad = _pad(kv, ((0, 0), (0, S), (0, 0)))
        kf_pad = _pad(ssm_k, ((0, 0), (0, S)))
        kv_f = (lambda x, axes=(-2,-1): torch.fft.rfft2(x.float(), dim=axes))(kv_pad[:, None, :, :], axes=(1, 2))
        kf_f = (lambda x, axes=(-2,-1): torch.fft.rfft2(x.float(), dim=axes))(kf_pad[:, None, :], axes=(1, 2))
        kf_f = (lambda x, *d: x.permute(*d))(kf_f, 1, 2, 0)
        y_f = kv_f * kf_f[None, :, :, :]
        y = (lambda x, s, axes=(-2,-1): torch.fft.irfft2(x, s=s, dim=axes).to(torch.bfloat16))(y_f, (1, L), axes=(1, 2))[:, 0, :S, :]
        y = y + kv * ssm_D[None, None, :]
        return q * y

    def _B_ref(x, w_gate, w_up, w_down):
        return (_gelu(x @ w_gate) * (x @ w_up)) @ w_down

    x = A_q
    g = _sigmoid((x * gate_w).sum(-1, keepdims=True))
    a = _A_ref(x, A_k, A_v, A_shift_k, A_ssm_k, A_ssm_D)
    b = _B_ref(x, B_w_gate, B_w_up, B_w_down)
    return g * a + (1 - g) * b
```

```cpp
#include <hip/hip_runtime.h>
#include <hip/hip_bf16.h>
#include <stdint.h>

typedef unsigned short u16;
static __device__ __forceinline__ float bf2f(u16 x) { return __uint_as_float(((unsigned)x) << 16); }
static __device__ __forceinline__ u16 f2bf(float f) {
    unsigned u = __float_as_uint(f);
    u += 0x7fffu + ((u >> 16) & 1u);
    return (u16)(u >> 16);
}
static __device__ __forceinline__ float rbf(float f) { return bf2f(f2bf(f)); }

constexpr int S = 2048, D = 512, F = 1376;

__global__ void k_kv(const u16* k, const u16* v, const u16* sk, u16* kvT) {
    int idx = blockIdx.x * 256 + threadIdx.x;
    int s = idx / D, d = idx % D;
    float acc = 0.f;
    for (int t = 0; t < 4; ++t) {
        int ss = s + t - 3;
        float kk = ss >= 0 ? bf2f(k[ss * D + d]) : 0.f;
        acc = rbf(acc + rbf(kk * bf2f(sk[d * 4 + 3 - t])));
    }
    kvT[d * S + s] = f2bf(acc * bf2f(v[s * D + d]));
}
__global__ void k_conv(const u16* kvT, const u16* ssmk, const u16* ssmD, u16* yT) {
    __shared__ float su[S], sh[S];
    int d = blockIdx.x;
    for (int i = threadIdx.x; i < S; i += 256) { su[i] = bf2f(kvT[d * S + i]); sh[i] = bf2f(ssmk[d * S + i]); }
    __syncthreads();
    for (int s = threadIdx.x; s < S; s += 256) {
        float acc = 0.f;
        for (int j = 0; j <= s; ++j) acc += su[j] * sh[s - j];
        float y = rbf(acc);
        y = rbf(y + rbf(su[s] * bf2f(ssmD[d])));
        yT[d * S + s] = f2bf(y);
    }
}
__global__ void k_up(const u16* x, const u16* wg, const u16* wu, u16* h) {
    __shared__ float sx[D];
    int s = blockIdx.x;
    for (int i = threadIdx.x; i < D; i += 256) sx[i] = bf2f(x[s * D + i]);
    __syncthreads();
    for (int f = threadIdx.x; f < F; f += 256) {
        float g = 0.f, u = 0.f;
        for (int kk = 0; kk < D; ++kk) { g += sx[kk] * bf2f(wg[kk * F + f]); u += sx[kk] * bf2f(wu[kk * F + f]); }
        g = rbf(g); u = rbf(u);
        float t = tanhf(0.7978845608028654f * (g + 0.044715f * g * g * g));
        float ge = rbf(0.5f * g * (1.f + t));
        h[s * F + f] = f2bf(ge * u);
    }
}
__global__ void k_down(const u16* h, const u16* wd, const u16* q, const u16* gw, const u16* yT, u16* out) {
    __shared__ float shh[F];
    __shared__ float red[256];
    int s = blockIdx.x;
    for (int i = threadIdx.x; i < F; i += 256) shh[i] = bf2f(h[s * F + i]);
    float p = 0.f;
    for (int i = threadIdx.x; i < D; i += 256) p += rbf(bf2f(q[s * D + i]) * bf2f(gw[i]));
    red[threadIdx.x] = p;
    __syncthreads();
    for (int o = 128; o > 0; o >>= 1) { if (threadIdx.x < o) red[threadIdx.x] += red[threadIdx.x + o]; __syncthreads(); }
    float z = rbf(red[0]);
    float g = rbf(1.f / (1.f + __expf(-z)));
    float omg = rbf(1.f - g);
    for (int d = threadIdx.x; d < D; d += 256) {
        float b = 0.f;
        for (int f = 0; f < F; ++f) b += shh[f] * bf2f(wd[f * D + d]);
        b = rbf(b);
        float a = rbf(bf2f(q[s * D + d]) * bf2f(yT[d * S + s]));
        out[s * D + d] = f2bf(rbf(g * a) + rbf(omg * b));
    }
}

extern "C" void kernel_launch(void* const* d_in, const int* in_sizes, int n_in,
                              void* d_out, int out_size, void* d_ws, size_t ws_size,
                              hipStream_t stream) {
    const u16* q = (const u16*)d_in[0];
    const u16* k = (const u16*)d_in[1];
    const u16* v = (const u16*)d_in[2];
    const u16* sk = (const u16*)d_in[3];
    const u16* ssmk = (const u16*)d_in[4];
    const u16* ssmD = (const u16*)d_in[5];
    const u16* wg = (const u16*)d_in[7];
    const u16* wu = (const u16*)d_in[8];
    const u16* wd = (const u16*)d_in[9];
    const u16* gw = (const u16*)d_in[10];
    u16* ws = (u16*)d_ws;
    u16* kvT = ws; u16* yT = ws + S * D; u16* h = ws + 2 * S * D;
    k_kv<<<S * D / 256, 256, 0, stream>>>(k, v, sk, kvT);
    k_conv<<<D, 256, 0, stream>>>(kvT, ssmk, ssmD, yT);
    k_up<<<S, 256, 0, stream>>>(q, wg, wu, h);
    k_down<<<S, 256, 0, stream>>>(h, wd, q, gw, yT, (u16*)d_out);
}
```

```cpp
#include <hip/hip_runtime.h>
#include <hip/hip_bf16.h>
#include <stdint.h>

typedef unsigned short u16;
static __device__ __forceinline__ float bf2f(u16 x) { return __uint_as_float(((unsigned)x) << 16); }
static __device__ __forceinline__ u16 f2bf(float f) {
    unsigned u = __float_as_uint(f);
    u += 0x7fffu + ((u >> 16) & 1u);
    return (u16)(u >> 16);
}
static __device__ __forceinline__ float rbf(float f) { return bf2f(f2bf(f)); }

constexpr int S = 2048, D = 512, F = 1376;

__global__ void k_kv(const u16* k, const u16* v, const u16* sk, u16* kvT) {
    int idx = blockIdx.x * 256 + threadIdx.x;
    int s = idx / D, d = idx % D;
    float acc = 0.f;
    for (int t = 0; t < 4; ++t) {
        int ss = s + t - 3;
        float kk = ss >= 0 ? bf2f(k[ss * D + d]) : 0.f;
        acc = rbf(acc + rbf(kk * bf2f(sk[d * 4 + 3 - t])));
    }
    kvT[d * S + s] = f2bf(acc * bf2f(v[s * D + d]));
}
__global__ void k_conv(const u16* kvT, const u16* ssmk, const u16* ssmD, u16* yT) {
    __shared__ float su[S], sh[S];
    int d = blockIdx.x;
    for (int i = threadIdx.x; i < S; i += 256) { su[i] = bf2f(kvT[d * S + i]); sh[i] = bf2f(ssmk[d * S + i]); }
    __syncthreads();
    for (int s = threadIdx.x; s < S; s += 256) {
        float acc = 0.f;
        for (int j = 0; j <= s; ++j) acc += su[j] * sh[s - j];
        float y = rbf(acc);
        y = rbf(y + rbf(su[s] * bf2f(ssmD[d])));
        yT[d * S + s] = f2bf(y);
    }
}
__global__ void k_up(const u16* x, const u16* wg, const u16* wu, u16* h) {
    __shared__ float sx[D];
    int s = blockIdx.x;
    for (int i = threadIdx.x; i < D; i += 256) sx[i] = bf2f(x[s * D + i]);
    __syncthreads();
    for (int f = threadIdx.x; f < F; f += 256) {
        float g = 0.f, u = 0.f;
        for (int kk = 0; kk < D; ++kk) { g += sx[kk] * bf2f(wg[kk * F + f]); u += sx[kk] * bf2f(wu[kk * F + f]); }
        g = rbf(g); u = rbf(u);
        float t = tanhf(0.7978845608028654f * (g + 0.044715f * g * g * g));
        float ge = rbf(0.5f * g * (1.f + t));
        h[s * F + f] = f2bf(ge * u);
    }
}
__global__ void k_down(const u16* h, const u16* wd, const u16* q, const u16* gw, const u16* yT, u16* out) {
    __shared__ float shh[F];
    __shared__ float red[256];
    int s = blockIdx.x;
    for (int i = threadIdx.x; i < F; i += 256) shh[i] = bf2f(h[s * F + i]);
    float p = 0.f;
    for (int i = threadIdx.x; i < D; i += 256) p += rbf(bf2f(q[s * D + i]) * bf2f(gw[i]));
    red[threadIdx.x] = p;
    __syncthreads();
    for (int o = 128; o > 0; o >>= 1) { if (threadIdx.x < o) red[threadIdx.x] += red[threadIdx.x + o]; __syncthreads(); }
    float z = rbf(red[0]);
    float g = rbf(1.f / (1.f + __expf(-z)));
    float omg = rbf(1.f - g);
    for (int d = threadIdx.x; d < D; d += 256) {
        float b = 0.f;
        for (int f = 0; f < F; ++f) b += shh[f] * bf2f(wd[f * D + d]);
        b = rbf(b);
        float a = rbf(bf2f(q[s * D + d]) * bf2f(yT[d * S + s]));
        out[s * D + d] = f2bf(rbf(g * a) + rbf(omg * b));
    }
}


__global__ void k_probe(const unsigned* p, unsigned* sink, int zero, int n, int stride_words) {
    unsigned idx = 0, acc = 0;
    for (int i = 0; i < n; ++i) {
        unsigned v = p[idx + threadIdx.x];
        acc += v;
        idx += stride_words + (v & zero);
    }
    if (acc == 0x12345678u && zero == 77) sink[0] = acc;
}

extern "C" void kernel_launch(void* const* d_in, const int* in_sizes, int n_in,
                              void* d_out, int out_size, void* d_ws, size_t ws_size,
                              hipStream_t stream) {
    const u16* q = (const u16*)d_in[0];
    const u16* k = (const u16*)d_in[1];
    const u16* v = (const u16*)d_in[2];
    const u16* sk = (const u16*)d_in[3];
    const u16* ssmk = (const u16*)d_in[4];
    const u16* ssmD = (const u16*)d_in[5];
    const u16* wg = (const u16*)d_in[7];
    const u16* wu = (const u16*)d_in[8];
    const u16* wd = (const u16*)d_in[9];
    const u16* gw = (const u16*)d_in[10];
    u16* ws = (u16*)d_ws;
    u16* kvT = ws; u16* yT = ws + S * D; u16* h = ws + 2 * S * D;
    k_probe<<<1, 1, 0, stream>>>((const unsigned*)(d_in[6]), (unsigned*)((char*)d_ws + (64<<20)), 0, 1, 1024);
    k_kv<<<S * D / 256, 256, 0, stream>>>(k, v, sk, kvT);
    k_conv<<<D, 256, 0, stream>>>(kvT, ssmk, ssmD, yT);
    k_up<<<S, 256, 0, stream>>>(q, wg, wu, h);
    k_down<<<S, 256, 0, stream>>>(h, wd, q, gw, yT, (u16*)d_out);
}
```

```cpp
#include <hip/hip_runtime.h>
#include <hip/hip_bf16.h>
#include <stdint.h>

typedef unsigned short u16;
static __device__ __forceinline__ float bf2f(u16 x) { return __uint_as_float(((unsigned)x) << 16); }
static __device__ __forceinline__ u16 f2bf(float f) {
    unsigned u = __float_as_uint(f);
    u += 0x7fffu + ((u >> 16) & 1u);
    return (u16)(u >> 16);
}
static __device__ __forceinline__ float rbf(float f) { return bf2f(f2bf(f)); }

constexpr int S = 2048, D = 512, F = 1376;

__global__ void k_kv(const u16* k, const u16* v, const u16* sk, u16* kvT) {
    int idx = blockIdx.x * 256 + threadIdx.x;
    int s = idx / D, d = idx % D;
    float acc = 0.f;
    for (int t = 0; t < 4; ++t) {
        int ss = s + t - 3;
        float kk = ss >= 0 ? bf2f(k[ss * D + d]) : 0.f;
        acc = rbf(acc + rbf(kk * bf2f(sk[d * 4 + 3 - t])));
    }
    kvT[d * S + s] = f2bf(acc * bf2f(v[s * D + d]));
}
__global__ void k_conv(const u16* kvT, const u16* ssmk, const u16* ssmD, u16* yT) {
    __shared__ float su[S], sh[S];
    int d = blockIdx.x;
    for (int i = threadIdx.x; i < S; i += 256) { su[i] = bf2f(kvT[d * S + i]); sh[i] = bf2f(ssmk[d * S + i]); }
    __syncthreads();
    for (int s = threadIdx.x; s < S; s += 256) {
        float acc = 0.f;
        for (int j = 0; j <= s; ++j) acc += su[j] * sh[s - j];
        float y = rbf(acc);
        y = rbf(y + rbf(su[s] * bf2f(ssmD[d])));
        yT[d * S + s] = f2bf(y);
    }
}
__global__ void k_up(const u16* x, const u16* wg, const u16* wu, u16* h) {
    __shared__ float sx[D];
    int s = blockIdx.x;
    for (int i = threadIdx.x; i < D; i += 256) sx[i] = bf2f(x[s * D + i]);
    __syncthreads();
    for (int f = threadIdx.x; f < F; f += 256) {
        float g = 0.f, u = 0.f;
        for (int kk = 0; kk < D; ++kk) { g += sx[kk] * bf2f(wg[kk * F + f]); u += sx[kk] * bf2f(wu[kk * F + f]); }
        g = rbf(g); u = rbf(u);
        float t = tanhf(0.7978845608028654f * (g + 0.044715f * g * g * g));
        float ge = rbf(0.5f * g * (1.f + t));
        h[s * F + f] = f2bf(ge * u);
    }
}
__global__ void k_down(const u16* h, const u16* wd, const u16* q, const u16* gw, const u16* yT, u16* out) {
    __shared__ float shh[F];
    __shared__ float red[256];
    int s = blockIdx.x;
    for (int i = threadIdx.x; i < F; i += 256) shh[i] = bf2f(h[s * F + i]);
    float p = 0.f;
    for (int i = threadIdx.x; i < D; i += 256) p += rbf(bf2f(q[s * D + i]) * bf2f(gw[i]));
    red[threadIdx.x] = p;
    __syncthreads();
    for (int o = 128; o > 0; o >>= 1) { if (threadIdx.x < o) red[threadIdx.x] += red[threadIdx.x + o]; __syncthreads(); }
    float z = rbf(red[0]);
    float g = rbf(1.f / (1.f + __expf(-z)));
    float omg = rbf(1.f - g);
    for (int d = threadIdx.x; d < D; d += 256) {
        float b = 0.f;
        for (int f = 0; f < F; ++f) b += shh[f] * bf2f(wd[f * D + d]);
        b = rbf(b);
        float a = rbf(bf2f(q[s * D + d]) * bf2f(yT[d * S + s]));
        out[s * D + d] = f2bf(rbf(g * a) + rbf(omg * b));
    }
}


typedef unsigned u32x4 __attribute__((ext_vector_type(4)));
template <int NT>
__global__ __launch_bounds__(512) void k_probe(const u32x4* p, unsigned* sink, int zero, int iters) {
    const u32x4* b = p + (size_t)blockIdx.x * iters * 4096 + threadIdx.x;
    u32x4 acc = {0,0,0,0};
    for (int it = 0; it < iters; ++it) {
        u32x4 v[8];
        #pragma unroll
        for (int i = 0; i < 8; ++i) v[i] = NT ? __builtin_nontemporal_load(b + it * 4096 + i * 512) : b[it * 4096 + i * 512];
        #pragma unroll
        for (int i = 0; i < 8; ++i) acc ^= v[i];
    }
    if (acc[0] == 0x12345678u && acc[1] == 1 && acc[2] == 2 && acc[3] == 3 && zero == 77) sink[0] = acc[0];
}

extern "C" void kernel_launch(void* const* d_in, const int* in_sizes, int n_in,
                              void* d_out, int out_size, void* d_ws, size_t ws_size,
                              hipStream_t stream) {
    const u16* q = (const u16*)d_in[0];
    const u16* k = (const u16*)d_in[1];
    const u16* v = (const u16*)d_in[2];
    const u16* sk = (const u16*)d_in[3];
    const u16* ssmk = (const u16*)d_in[4];
    const u16* ssmD = (const u16*)d_in[5];
    const u16* wg = (const u16*)d_in[7];
    const u16* wu = (const u16*)d_in[8];
    const u16* wd = (const u16*)d_in[9];
    const u16* gw = (const u16*)d_in[10];
    u16* ws = (u16*)d_ws;
    u16* kvT = ws; u16* yT = ws + S * D; u16* h = ws + 2 * S * D;
    k_probe<0><<<256, 512, 0, stream>>>((const u32x4*)((char*)d_ws + (64<<20)), (unsigned*)((char*)d_ws + (60<<20)), 0, 1);
    k_kv<<<S * D / 256, 256, 0, stream>>>(k, v, sk, kvT);
    k_conv<<<D, 256, 0, stream>>>(kvT, ssmk, ssmD, yT);
    k_up<<<S, 256, 0, stream>>>(q, wg, wu, h);
    k_down<<<S, 256, 0, stream>>>(h, wd, q, gw, yT, (u16*)d_out);
}
```

```cpp
#include <hip/hip_runtime.h>
#include <stdint.h>

typedef unsigned short u16;
typedef unsigned int u32;
typedef unsigned long long u64;
typedef short s16x4 __attribute__((ext_vector_type(4)));
typedef __bf16 bf16x8 __attribute__((ext_vector_type(8)));
typedef __bf16 bf16x2 __attribute__((ext_vector_type(2)));
typedef float f32x2 __attribute__((ext_vector_type(2)));
typedef float f32x4 __attribute__((ext_vector_type(4)));
typedef float f32x16 __attribute__((ext_vector_type(16)));
typedef u32 u32x2 __attribute__((ext_vector_type(2)));
typedef u32 u32x4 __attribute__((ext_vector_type(4)));
#define LAS __attribute__((address_space(3)))
#define GAS __attribute__((address_space(1)))
#define DI __device__ __forceinline__
#define RLX_AGENT __ATOMIC_RELAXED, __HIP_MEMORY_SCOPE_AGENT
#define SC1 16

constexpr int S = 2048, D = 512, F = 1376;

constexpr u32 WS_EPOCH = 0;
constexpr u32 WS_F0 = 4096;
constexpr u32 WS_F1 = 8192;
constexpr u32 WS_F2 = 16384;
constexpr u32 WS_G = 24576;
constexpr u32 WS_KV = 1u << 20;
constexpr u32 WS_Y = 3u << 20;
constexpr u32 WS_H = 5u << 20;
constexpr u32 WS_END = WS_H + 64 * 86 * 1024;
constexpr u32 WS_DBG = 16u << 20;

constexpr u32 L_X = 0;
constexpr u32 L_WG = 49152;
constexpr u32 L_WU = 86016 + 32;
constexpr u32 L_TOEP = 122912;
constexpr u32 TOEP_CP = 4160;
constexpr u32 L_SPARE = L_TOEP + 8 * TOEP_CP;
constexpr u32 L_CTL = L_SPARE + 4096;
constexpr u32 L_U = L_WG + 2 * 12288;
constexpr u32 U_CH = 4608;
constexpr u32 LDS_BYTES = 163840;
constexpr u32 C_LANDED = 0;
constexpr u32 C_DONE = 16;
constexpr u32 C_TREADY = 32;
constexpr u32 C_CDONE = 48;
constexpr u32 C_WDA = 64;
constexpr u32 C_WDB = 80;
constexpr u32 C_WDC = 96;
constexpr u32 C_G2 = 112;
constexpr u32 C_HRDY = 128;
constexpr u32 C_S0 = 160;
constexpr u32 C_TAG = 144;

__host__ __device__ constexpr u32 slot_addr(int j) {
    if (j < 4) return L_X + 2 * 16384 + j * 4096;
    if (j < 7) return L_WU + 2 * 12288 + (j - 4) * 4096;
    if (j < 8) return L_SPARE;
    if (j < 12) return L_X + (j - 8) * 4096;
    if (j < 15) return L_WG + (j - 12) * 4096;
    if (j < 18) return L_WU + (j - 15) * 4096;
    if (j < 22) return L_X + 16384 + (j - 18) * 4096;
    if (j < 25) return L_WG + 12288 + (j - 22) * 4096;
    if (j < 28) return L_WU + 12288 + (j - 25) * 4096;
    if (j < 31) return L_U + (j - 28) * 4096;
    if (j < 39) return L_TOEP + (j - 31) * 4096;
    return L_X + 2 * 16384 + (j - 39) * 4096;
}
constexpr int NA = 28, NB = 39, NSTEP = 43;
constexpr int G2_MARK = 22;
constexpr u32 L_YST = L_X;
constexpr u32 L_GA = L_X + 8192;
constexpr u32 L_EXCH = L_X + 16384;

#define MPRIO 0
#define LPRIO 1
#define HPDELAY 0
#define TOUCH 0
constexpr u32 OOB = 0x7ffffff0u;
constexpr u32 SPIN_CAP = 1u << 16;

struct Args {
    const u16 *q, *k, *v, *sk, *ssmk, *ssmD, *wg, *wu, *wd, *gw;
    u16* out;
    char* ws;
};

#define TS(i) do { } while (0)
#define ACC_BEGIN() do { } while (0)
#define ACC(i) do { } while (0)

DI float bflo(u32 x) { return __uint_as_float(x << 16); }
DI float bfhi(u32 x) { return __uint_as_float(x & 0xffff0000u); }
DI u32 pk2(float a, float b) { f32x2 p = {a, b}; return __builtin_bit_cast(u32, __builtin_convertvector(p, bf16x2)); }
DI float rbf(float a) { return bflo(pk2(a, a)); }
DI u32 rfl(u32 x) { return __builtin_amdgcn_readfirstlane(x); }
DI LAS char* lp(u32 off) { return (LAS char*)(uintptr_t)off; }
DI u32x4 lds_r128(u32 a) { return *(LAS u32x4*)lp(a); }
DI u32x2 lds_r64(u32 a) { return *(LAS u32x2*)lp(a); }
DI void lds_w128(u32 a, u32x4 v) { *(LAS u32x4*)lp(a) = v; }
DI u32x2 lds_tr(u32 a) { return __builtin_bit_cast(u32x2, __builtin_amdgcn_ds_read_tr16_b64_v4i16((LAS s16x4*)lp(a))); }
DI bf16x8 cat(u32x2 a, u32x2 b) { u32x4 v = {a[0], a[1], b[0], b[1]}; return __builtin_bit_cast(bf16x8, v); }
DI bf16x8 asb(u32x4 v) { return __builtin_bit_cast(bf16x8, v); }
#define CFENCE() asm volatile("" ::: "memory")
#define WAIT_VM(n) asm volatile("s_waitcnt vmcnt(%0)" ::"i"(n) : "memory")
#define WAIT_LGKM0() asm volatile("s_waitcnt lgkmcnt(0)" ::: "memory")
DI void ctl_set(u32 off, u32 v) { CFENCE(); *(volatile LAS u32*)lp(L_CTL + off) = v; CFENCE(); }
DI u32 ctl_min2(u32 off) { u32x2 v = *(volatile LAS u32x2*)lp(L_CTL + off); return min(rfl(v[0]), rfl(v[1])); }
DI u32 ctl_min4(u32 off) { u32x4 v = *(volatile LAS u32x4*)lp(L_CTL + off); return min(min(rfl(v[0]), rfl(v[1])), min(rfl(v[2]), rfl(v[3]))); }
DI u32 ctl_get(u32 off) { return rfl(*(volatile LAS u32*)lp(L_CTL + off)); }
#define LDS_WAIT(expr_ge) do { u32 _sp = 0; while (!(expr_ge)) { __builtin_amdgcn_s_sleep(1); if (++_sp > SPIN_CAP) break; } CFENCE(); } while (0)

DI void poll_flags(const char* ws, u32 byte_off, u64 tag) {
    const GAS u64* p = (const GAS u64*)(ws + byte_off);
#pragma unroll 1
    for (u32 sp = 0; sp < SPIN_CAP; ++sp) {
        u64 x = __hip_atomic_load(p, RLX_AGENT);
        if (__all(x == tag)) break;
        __builtin_amdgcn_s_sleep(16);
    }
    CFENCE();
}
DI void set_flag(char* ws, u32 byte_off, u64 tag, int lane) {
    if (lane == 0) __hip_atomic_store((GAS u64*)(ws + byte_off), tag, RLX_AGENT);
}
DI u64 load_epoch(const char* ws) { return __hip_atomic_load((const GAS u64*)(ws + WS_EPOCH), RLX_AGENT); }
DI u64 make_tag(u64 e) {
    u32 lo = rfl((u32)e), hi = rfl((u32)(e >> 32));
    return (((u64)hi << 32) | lo) + 0x9E3779B97F4A7C15ull;
}
DI u64 lds_tag() { u32x2 v = *(volatile LAS u32x2*)lp(L_CTL + C_TAG); return ((u64)rfl(v[1]) << 32) | rfl(v[0]); }
typedef __amdgpu_buffer_rsrc_t rsrc_t;
DI rsrc_t mk_rsrc(const void* p, u32 bytes) { return __builtin_amdgcn_make_buffer_rsrc((void*)p, 0, bytes, 0x00020000); }
DI void touch(rsrc_t r, u32 voff, u32 soff) { __builtin_amdgcn_raw_ptr_buffer_load_lds(r, (LAS void*)lp(L_CTL + 1024), 4, voff, soff, 0, 0); }
DI void dma16(rsrc_t r, u32 lds, u32 voff, u32 soff) { __builtin_amdgcn_raw_ptr_buffer_load_lds(r, (LAS void*)lp(lds), 16, voff, soff, 0, 0); }
#define SGB(mask, n) __builtin_amdgcn_sched_group_barrier(mask, n, 0)
#define SB0() __builtin_amdgcn_sched_barrier(0)
#define MFMA32(a, b, c) __builtin_amdgcn_mfma_f32_32x32x16_bf16(a, b, c, 0, 0, 0)
#define MFMA16(a, b, c) __builtin_amdgcn_mfma_f32_16x16x32_bf16(a, b, c, 0, 0, 0)

DI void wd_dma(rsrc_t rwd, int j, int i, u32 voff, u32 col_bytes) {
    const int kh = i >> 1, half = i & 1;
    dma16(rwd, slot_addr(j) + i * 1024, voff, (u32)((kh * NSTEP + j) * 16 + half * 8) * 1024u + col_bytes);
}

template <int PROG>
DI void conv_prog(int ch, int lane, int d, float Dv, rsrc_t rws) {
    constexpr int NP = PROG == 0 ? 8 : 6;
    constexpr int NT = PROG == 0 ? 3 : 5;
    constexpr int TL[5] = {PROG == 0 ? 7 : 5, PROG == 0 ? 6 : 4, PROG == 0 ? 2 : 3, 1, 0};
    const int r = lane & 15, kg = lane >> 4;
    const int w0 = 9 + r - 8 * kg;
    const int sg = (w0 + 16) & 3;
    u32 aaddr = L_TOEP + (u32)(ch * 4 + sg) * TOEP_CP + (u32)(w0 + 16 - sg) * 2;
    u32 baddr = L_U + (u32)ch * U_CH + 480 + (u32)(8 * kg + 16 * r - 16) * 2;
    f32x4 acc[NT];
#pragma unroll
    for (int i = 0; i < NT; ++i) acc[i] = (f32x4){0.f, 0.f, 0.f, 0.f};
    bf16x8 A[2][NP], B[2][NP];
    auto ld = [&](int buf) {
#pragma unroll
        for (int p = 0; p < NP; ++p) {
            u32x2 lo = *(volatile LAS u32x2*)lp(aaddr + p * 512), hi = *(volatile LAS u32x2*)lp(aaddr + p * 512 + 8);
            A[buf][p] = cat(lo, hi);
            B[buf][NP - 1 - p] = asb(lds_r128(baddr + (NP - 1 - p) * 512));
        }
        aaddr += 64;
        baddr -= 64;
        asm volatile("" : "+v"(aaddr), "+v"(baddr));
    };
    auto mm = [&](int buf) {
#pragma unroll
        for (int p = 0; p < NP; ++p)
#pragma unroll
            for (int i = 0; i < NT; ++i)
                if (p <= TL[i]) acc[i] = MFMA16(A[buf][p], B[buf][TL[i] - p], acc[i]);
    };
    auto pat = [&]() {
#pragma unroll
        for (int i = 0; i < NP; ++i) { SGB(0x8, 1); SGB(0x100, 2); SGB(0x8, 1); SGB(0x100, 1); }
        SGB(0x8, 18 - 2 * NP);
    };
    ld(0);
    SB0();
#pragma unroll 1
    for (int rho = 0; rho < 8; rho += 2) {
        ld(1); mm(0); pat(); SB0();
        ld(0); mm(1); pat(); SB0();
    }
    const u32 uaddr = L_U + (u32)ch * U_CH + 480 + (u32)(16 * r + 8 * (kg >> 1) + 4 * (1 - (kg & 1))) * 2;
#pragma unroll
    for (int i = 0; i < NT; ++i) {
        u32x2 u = lds_r64(uaddr + TL[i] * 512);
        float y0 = rbf(acc[i][0]) + rbf(bfhi(u[1]) * Dv);
        float y1 = rbf(acc[i][1]) + rbf(bflo(u[1]) * Dv);
        float y2 = rbf(acc[i][2]) + rbf(bfhi(u[0]) * Dv);
        float y3 = rbf(acc[i][3]) + rbf(bflo(u[0]) * Dv);
        u32x2 o = {pk2(y0, y1), pk2(y2, y3)};
        __builtin_amdgcn_raw_buffer_store_b64(o, rws, (u32)(32 * r + 8 * kg), WS_Y + (u32)d * 4096u + TL[i] * 512, SC1);
    }
}

__global__ __launch_bounds__(512, 2) void fused_kernel(Args a) {
    __shared__ __attribute__((aligned(128))) char smem[LDS_BYTES];
    const int tid = threadIdx.x, lane = tid & 63;
    const int wave = rfl(tid >> 6);
    const int bid = blockIdx.x, xcd = bid & 7, l = bid >> 3;
    TS(0);
    if (tid < 48) ((volatile LAS u32*)((LAS char*)smem + L_CTL))[tid] = 0;

    const int rt = (xcd & 1) * 8 + (l & 7), cq = xcd >> 1, cj = l >> 3, ct = cq * 4 + cj;
    const int unit0 = 11 * cq + 3 * cj;
    const int nunits = min(3, min(11 * cq + 11, 43) - unit0);
    const int row2 = (xcd & 1) * 1024 + (xcd >> 1) * 256 + (l & 3) * 64, c2 = l >> 2;

    const rsrc_t rws = mk_rsrc(a.ws, WS_END);
    const rsrc_t rq = mk_rsrc(a.q, S * D * 2);
    const rsrc_t rwd = mk_rsrc(a.wd, F * D * 2);

    const u32 xv0 = (u32)(lane >> 3) * 1024 + (u32)(((lane & 7) ^ (lane >> 4)) * 16);
    const u32 xv1 = xv0 ^ 64;
    u32 wv[3];
#pragma unroll
    for (int m = 0; m < 3; ++m) {
        int n = 64 * m + lane, rr = n / 12, c = n - rr * 12;
        wv[m] = c < 4 * nunits ? (u32)(rr * (F * 2) + c * 16) : OOB;
    }
    const rsrc_t rw = mk_rsrc((wave & 1) ? a.wu : a.wg, F * D * 2);
    const u32 lwb = (wave & 1) ? L_WU : L_WG, ws0 = (u32)unit0 * 64;
    asm volatile("s_waitcnt lgkmcnt(0)\n\ts_barrier" ::: "memory");
    if (wave < 4) {
        __builtin_amdgcn_s_setprio(MPRIO);
        {
            const u32 xs = (u32)(rt * 128 + wave * 32) * 1024, wq = (u32)(wave >> 1);
#pragma unroll
            for (int i = 0; i < 4; ++i) dma16(rq, L_X + wave * 4096 + i * 1024, (i & 1) ? xv1 : xv0, xs + i * 8192);
#pragma unroll
            for (int i = 0; i < 6; ++i) dma16(rw, lwb + wq * 6144 + i * 1024, wv[i % 3], ws0 + (u32)(wq * 32 + (i / 3) * 16) * (F * 2));
        }
        const int sw = wave & 1, fw = wave >> 1;
        const int r32 = lane & 31, hh = lane >> 5;
        f32x16 acc[2][3];
#pragma unroll
        for (int i = 0; i < 2; ++i)
#pragma unroll
            for (int j = 0; j < 3; ++j)
#pragma unroll
                for (int e = 0; e < 16; ++e) acc[i][j][e] = 0.f;
        const u32 xa0 = L_X + (u32)(64 * sw + r32) * 128 + (u32)((hh ^ ((r32 >> 1) & 7)) * 16);
        const u32 wa0 = ((lane >> 4) & 1 ? L_WU : L_WG) + (u32)(8 * hh + ((lane & 15) >> 2)) * 192 + (u32)(3 * fw * 32 + (lane & 3) * 8);
        bf16x8 xb[2][2], wb[2][3];
        auto ldfrag = [&](int buf, u32 xa, u32 wa, int kk) {
#pragma unroll
            for (int i = 0; i < 2; ++i) xb[buf][i] = asb(lds_r128((xa ^ (u32)(kk << 5)) + i * 4096));
#pragma unroll
            for (int j = 0; j < 3; ++j) wb[buf][j] = cat(lds_tr(wa + kk * 16 * 192 + j * 32), lds_tr(wa + kk * 16 * 192 + 4 * 192 + j * 32));
        };
        auto mm = [&](int buf) {
#pragma unroll
            for (int j = 0; j < 3; ++j)
#pragma unroll
                for (int i = 0; i < 2; ++i) acc[i][j] = MFMA32(wb[buf][j], xb[buf][i], acc[i][j]);
        };
#pragma unroll
        for (int i = 0; i < 2; ++i)
#pragma unroll
            for (int j = 0; j < 3; ++j) asm volatile("" : "+v"(acc[i][j]));
        WAIT_VM(0);
        ctl_set(C_S0 + wave * 4, 1);
        LDS_WAIT(ctl_min4(C_S0) >= 1);
        TS(1);
        WAIT_LGKM0();
        u32 xa = xa0, wa = wa0;
        int b = 0;
        auto pat = [&]() { SGB(0x8, 1); SGB(0x100, 2); SGB(0x8, 1); SGB(0x100, 2); SGB(0x8, 1); SGB(0x100, 2); SGB(0x8, 1); SGB(0x100, 2); SGB(0x8, 2); };
        ldfrag(0, xa, wa, 0);
        SB0();
#pragma unroll 1
        for (int s = 0; s < 8; ++s) {
            ldfrag(1, xa, wa, 1); mm(0); pat(); SB0();
            ldfrag(0, xa, wa, 2); mm(1); pat(); SB0();
            u32x2 lv = *(volatile LAS u32x2*)lp(L_CTL + C_LANDED);
            SB0();
            ldfrag(1, xa, wa, 3);
            *(volatile LAS u32*)lp(L_CTL + C_DONE + wave * 4) = s + 1;
            mm(0);
            SGB(0x8, 1); SGB(0x100, 2); SGB(0x8, 1); SGB(0x100, 2); SGB(0x8, 1); SGB(0x100, 2); SGB(0x8, 1); SGB(0x100, 2); SGB(0x8, 1); SGB(0x200, 1); SGB(0x8, 1);
            SB0();
            b = b == 2 ? 0 : b + 1;
            xa = xa0 + b * 16384; wa = wa0 + b * 12288;
            const u32 need = min(s + 2, 8);
            if (min(rfl(lv[0]), rfl(lv[1])) < need) { ACC_BEGIN(); LDS_WAIT(ctl_min2(C_LANDED) >= need); ACC(0); }
            CFENCE();
            ldfrag(0, xa, wa, 0);
            mm(1); pat(); SB0();
        }
        TS(2);
        __builtin_amdgcn_s_setprio(2);
        {
            const float c0 = -2.f * 0.7978845608028654f * 1.4426950408889634f, c1 = c0 * 0.044715f;
#pragma unroll
            for (int j = 0; j < 3; ++j) {
                const int ftl = 3 * fw + j;
                if (ftl < 2 * nunits) {
#pragma unroll
                    for (int i = 0; i < 2; ++i) {
                        u32x4 o;
#pragma unroll
                        for (int e = 0; e < 4; ++e) {
                            const f32x2 g = {acc[i][j][2 * e], acc[i][j][2 * e + 1]}, u = {acc[i][j][8 + 2 * e], acc[i][j][9 + 2 * e]};
                            const f32x2 z = g * __builtin_elementwise_fma(g * g, (f32x2){c1, c1}, (f32x2){c0, c0});
                            const f32x2 dn = (f32x2){__builtin_amdgcn_exp2f(z[0]), __builtin_amdgcn_exp2f(z[1])} + (f32x2){1.f, 1.f};
                            const f32x2 hv = (g * u) * (f32x2){__builtin_amdgcn_rcpf(dn[0]), __builtin_amdgcn_rcpf(dn[1])};
                            o[e] = pk2(hv[0], hv[1]);
                        }
                        const int st = rt * 4 + sw * 2 + i, ft = unit0 * 2 + ftl;
                        __builtin_amdgcn_raw_buffer_store_b128(o, rws, (u32)lane * 16u + WS_H + (u32)(st * 86 + ft) * 1024u, 0, SC1);
                    }
                }
            }
        }
        TS(3);
        LDS_WAIT(ctl_min2(C_TREADY) >= 1);
        const u64 tag = lds_tag();
        WAIT_VM(0);
        set_flag(a.ws, WS_F1 + (u32)(((rt * 2 + sw) * 16 + ct) * 2 + fw) * 8u, tag, lane);
        TS(4);
        const int kh = wave >> 1;
        if (wave == 0) {
            poll_flags(a.ws, WS_F1 + (u32)(row2 >> 6) * 256u + (u32)(lane & 31) * 8u, tag);
            ctl_set(C_HRDY, 1);
        } else {
            LDS_WAIT(ctl_get(C_HRDY) >= 1);
        }
        TS(5);
        constexpr int HWIN = 16;
        u32x4 hb[NSTEP];
        const u32 hbase = WS_H + (u32)(((row2 >> 5) + sw) * 86 + kh * NSTEP) * 1024u;
#define HLOAD(j) hb[j] = __builtin_amdgcn_raw_buffer_load_b128(rws, (u32)lane * 16u + (j) * 1024, hbase, SC1)
#pragma unroll
        for (int j = 0; j < HWIN; ++j) HLOAD(j);
        const float gval = __builtin_bit_cast(float, __builtin_amdgcn_raw_buffer_load_b32(rws, (u32)(row2 + 32 * sw + r32) * 4u, WS_G, SC1));
        f32x16 c2a[2];
#pragma unroll
        for (int i = 0; i < 2; ++i)
#pragma unroll
            for (int e = 0; e < 16; ++e) c2a[i][e] = 0.f;
        const int q4 = (lane & 15) >> 2, p4 = lane & 3, g1 = (lane >> 4) & 1;
        const int drel = 16 * (p4 & 1) + 4 * (p4 >> 1) + 8 * g1;
        const u32 swz = (u32)((q4 >> 1) & 1) << 6;
        const u32 da0 = (u32)kh * 2048 + (u32)(4 * hh + q4) * 128 + (((u32)drel * 2) ^ swz);
        const u32 da1 = da0 ^ 64;
        bf16x8 a0[2], a1[2];
#define ALOAD(j) do { a0[(j) & 1] = cat(lds_tr(da0 + slot_addr(j)), lds_tr(da0 + slot_addr(j) + 1024)); \
                      a1[(j) & 1] = cat(lds_tr(da1 + slot_addr(j)), lds_tr(da1 + slot_addr(j) + 1024)); } while (0)
#define G2_RANGE(j0, j1, LASTLD) _Pragma("unroll") for (int j = j0; j < j1; ++j) { \
            if (j + HWIN < NSTEP) HLOAD(j + HWIN); \
            if (j + 1 < j1 || LASTLD) ALOAD(j + 1); \
            c2a[0] = MFMA32(a0[j & 1], asb(hb[j]), c2a[0]); \
            c2a[1] = MFMA32(a1[j & 1], asb(hb[j]), c2a[1]); \
            SGB(0x8, 1); SGB(0x100, 2); SGB(0x20, 1); SGB(0x8, 1); SGB(0x100, 2); SB0(); }
        LDS_WAIT(ctl_min2(C_WDA) >= 1);
        ALOAD(0);
        SB0();
        G2_RANGE(0, G2_MARK, true)
        ctl_set(C_G2 + wave * 4, 1);
        G2_RANGE(G2_MARK, NA - 1, true)
        LDS_WAIT(ctl_min4(C_WDB) >= 1);
        G2_RANGE(NA - 1, NB - 1, true)
        LDS_WAIT(ctl_min4(C_WDC) >= 1);
        G2_RANGE(NB - 1, NSTEP, false)
        TS(6);
        {
            LDS_WAIT(ctl_min4(C_G2) >= 1);
            const f32x16 snd = kh ? c2a[0] : c2a[1];
            f32x16 keep = kh ? c2a[1] : c2a[0];
#pragma unroll
            for (int e = 0; e < 4; ++e) {
                f32x4 t = {snd[4 * e], snd[4 * e + 1], snd[4 * e + 2], snd[4 * e + 3]};
                lds_w128(L_EXCH + (u32)wave * 4096 + e * 1024 + lane * 16, __builtin_bit_cast(u32x4, t));
            }
            asm volatile("s_waitcnt lgkmcnt(0)\n\ts_barrier" ::: "memory");
            TS(7);
            const float omg = rbf(1.f - gval);
            u32x4 ga0 = lds_r128(L_GA + (u32)wave * 2048 + lane * 16), ga1 = lds_r128(L_GA + (u32)wave * 2048 + 1024 + lane * 16);
            u32 o[8];
#pragma unroll
            for (int e = 0; e < 4; ++e) {
                f32x4 t = __builtin_bit_cast(f32x4, lds_r128(L_EXCH + (u32)(wave ^ 2) * 4096 + e * 1024 + lane * 16));
                u32 gA = e < 2 ? ga0[2 * e] : ga1[2 * e - 4], gB = e < 2 ? ga0[2 * e + 1] : ga1[2 * e - 3];
                o[2 * e] = pk2(__builtin_fmaf(omg, keep[4 * e] + t[0], bflo(gA)), __builtin_fmaf(omg, keep[4 * e + 1] + t[1], bfhi(gA)));
                o[2 * e + 1] = pk2(__builtin_fmaf(omg, keep[4 * e + 2] + t[2], bflo(gB)), __builtin_fmaf(omg, keep[4 * e + 3] + t[3], bfhi(gB)));
            }
            GAS u32x4* op = (GAS u32x4*)(a.out + (size_t)(row2 + 32 * sw + r32) * D + c2 * 64 + 32 * kh + 16 * hh);
            op[0] = (u32x4){o[0], o[1], o[2], o[3]};
            op[1] = (u32x4){o[4], o[5], o[6], o[7]};
        }
        TS(8);
    } else {
        __builtin_amdgcn_s_setprio(LPRIO);
        const int lw = wave - 4;
        const int ch = lw & 1;
        const int dch = xcd * 64 + 2 * l + ch;
        const u32 wdv = (u32)(lane >> 3) * 1024 + (u32)(((lane & 7) ^ (((lane >> 4) & 1) << 2)) * 16);
        const u32 wdcol = (u32)c2 * 128;
        u64 tag;
        if (lw < 2) {
            const u32 xs0 = (u32)(rt * 128 + lw * 64) * 1024;
            const u32 lx = L_X + lw * 8192;
            const u32 txv = lane < 16 ? (u32)((xcd & 1) * 1024 + l * 32 + lw * 16 + lane) * 1024u : OOB;
            u32 twv;
            { const int u = l * 14 + lane, r = u / 7, c = u - r * 7; twv = lane < 14 ? (u32)(r * (F * 2) + cq * 704 + c * 128 - (c == 6 ? 64 : 0)) : OOB; }
            const u32 tdv = (u32)((l * 2 + lw) * 172 + lane) * 128u;
            auto issue_a = [&](int s, int b) {
#pragma unroll
                for (int i = 0; i < 8; ++i) dma16(rq, lx + b * 16384 + i * 1024, (i & 1) ? xv1 : xv0, xs0 + i * 8192 + s * 128);
#pragma unroll
                for (int i = 0; i < 2; ++i) dma16(rw, lwb + b * 12288 + i * 1024, wv[i % 3], ws0 + (u32)(s * 64 + (i / 3) * 16) * (F * 2));
            };
            auto issue_b = [&](int s, int b) {
#pragma unroll
                for (int i = 2; i < 12; ++i) dma16(rw, lwb + b * 12288 + i * 1024, wv[i % 3], ws0 + (u32)(s * 64 + (i / 3) * 16) * (F * 2));
            };
            issue_a(1, 1); issue_b(1, 1);
            issue_a(2, 2);
            WAIT_VM(10);
            ctl_set(C_LANDED + lw * 4, 2);
            TS(1);
            issue_b(2, 2);
            int b = 0;
#pragma unroll 1
            for (int s = 0; s < 5; ++s) {
                ACC_BEGIN();
                LDS_WAIT(ctl_min4(C_DONE) >= (u32)(s + 1));
                ACC(1);
                issue_a(s + 3, b);
                ACC(2);
                WAIT_VM(10);
                ACC(0);
                ctl_set(C_LANDED + lw * 4, s + 3);
                issue_b(s + 3, b);
                ACC(2);
                b = b == 2 ? 0 : b + 1;
            }
            WAIT_VM(2 * TOUCH);
            ctl_set(C_LANDED + lw * 4, 8);
            TS(2);
            LDS_WAIT(ctl_min4(C_DONE) >= 6);
#pragma unroll
            for (int j = 0; j < 8; ++j) { wd_dma(rwd, j, 2 * lw, wdv, wdcol); wd_dma(rwd, j, 2 * lw + 1, wdv, wdcol); }
            LDS_WAIT(ctl_min4(C_DONE) >= 7);
#pragma unroll
            for (int j = 8; j < 18; ++j) { wd_dma(rwd, j, 2 * lw, wdv, wdcol); wd_dma(rwd, j, 2 * lw + 1, wdv, wdcol); }
            LDS_WAIT(ctl_min4(C_DONE) >= 8);
#pragma unroll
            for (int j = 18; j < NA; ++j) { wd_dma(rwd, j, 2 * lw, wdv, wdcol); wd_dma(rwd, j, 2 * lw + 1, wdv, wdcol); }
            WAIT_VM(0);
            ctl_set(C_WDA + lw * 4, 1);
            TS(3);
            LDS_WAIT(ctl_get(C_TREADY + ch * 4) >= 2);
            tag = lds_tag();
        } else {
            const rsrc_t rk = mk_rsrc(a.k, S * D * 2), rv = mk_rsrc(a.v, S * D * 2);
            const int R0 = l * 64 + ch * 32, cg = lane & 7, rq4 = lane >> 3;
            u32x4 kk[7], vv[4], sw4[4];
#pragma unroll
            for (int i = 0; i < 7; ++i) {
                int row = R0 + 4 * rq4 + i - 3;
                u32 off = row >= 0 ? (u32)(row * 1024 + xcd * 128 + cg * 16) : OOB;
                kk[i] = __builtin_amdgcn_raw_buffer_load_b128(rk, off, 0, 0);
            }
#pragma unroll
            for (int i = 0; i < 4; ++i) vv[i] = __builtin_amdgcn_raw_buffer_load_b128(rv, (u32)((R0 + 4 * rq4 + i) * 1024 + xcd * 128 + cg * 16), 0, 0);
#pragma unroll
            for (int i = 0; i < 4; ++i) sw4[i] = *(const GAS u32x4*)(a.sk + (xcd * 64 + cg * 8) * 4 + i * 8);
            u32x4 qg[4];
            const int grow = rt * 128 + ct * 8 + ch * 4;
#pragma unroll
            for (int i = 0; i < 4; ++i) qg[i] = __builtin_amdgcn_raw_buffer_load_b128(rq, (u32)lane * 16u, (u32)(grow + i) * 1024u, 0);
            const u32x4 gwv = *(const GAS u32x4*)(a.gw + lane * 8);
            const rsrc_t rh = mk_rsrc(a.ssmk + (size_t)dch * S, S * 2);
            u32x4 hc[4]; u32x2 hn[4];
#pragma unroll
            for (int i = 0; i < 4; ++i) {
                hc[i] = __builtin_amdgcn_raw_buffer_load_b128(rh, (u32)(lane * 16 + i * 1024), 0, 0);
                hn[i] = __builtin_amdgcn_raw_buffer_load_b64(rh, (u32)(lane * 16 + i * 1024 + 16), 0, 0);
            }
            const u64 ep = load_epoch(a.ws);
            WAIT_VM(0);
            TS(10);
            const u32 scr = L_TOEP + (u32)ch * 4 * TOEP_CP;
#pragma unroll
            for (int i = 0; i < 4; ++i) {
                u32x4 o;
#pragma unroll
                for (int c = 0; c < 4; ++c) {
                    float e0 = bflo(kk[i][c]) * bfhi(sw4[c][1]);
                    e0 = __builtin_fmaf(bflo(kk[i + 1][c]), bflo(sw4[c][1]), e0);
                    e0 = __builtin_fmaf(bflo(kk[i + 2][c]), bfhi(sw4[c][0]), e0);
                    e0 = __builtin_fmaf(bflo(kk[i + 3][c]), bflo(sw4[c][0]), e0);
                    float e1 = bfhi(kk[i][c]) * bfhi(sw4[c][3]);
                    e1 = __builtin_fmaf(bfhi(kk[i + 1][c]), bflo(sw4[c][3]), e1);
                    e1 = __builtin_fmaf(bfhi(kk[i + 2][c]), bfhi(sw4[c][2]), e1);
                    e1 = __builtin_fmaf(bfhi(kk[i + 3][c]), bflo(sw4[c][2]), e1);
                    o[c] = pk2(e0 * bflo(vv[i][c]), e1 * bfhi(vv[i][c]));
                }
                lds_w128(scr + (u32)(4 * rq4 + i) * 128 + cg * 16, o);
            }
            CFENCE();
            {
                const int gq = lane >> 4, q4 = (lane & 15) >> 2, p4 = lane & 3;
#pragma unroll
                for (int i = 0; i < 4; ++i) {
                    u32x2 t0 = lds_tr(scr + (u32)(8 * gq + 7 - q4) * 128 + i * 32 + p4 * 8);
                    u32x2 t1 = lds_tr(scr + (u32)(8 * gq + 3 - q4) * 128 + i * 32 + p4 * 8);
                    u32x4 o = {t0[0], t0[1], t1[0], t1[1]};
                    __builtin_amdgcn_raw_buffer_store_b128(o, rws, (u32)((lane & 15) * 64 + gq * 16) + WS_KV + (u32)((R0 >> 5) * 512 + xcd * 64 + i * 16) * 64u, 0, SC1);
                }
            }
            TS(11);
            {
                float z[4];
#pragma unroll
                for (int i = 0; i < 4; ++i) {
                    float s = 0.f;
#pragma unroll
                    for (int c = 0; c < 4; ++c) { s += rbf(bflo(qg[i][c]) * bflo(gwv[c])); s += rbf(bfhi(qg[i][c]) * bfhi(gwv[c])); }
#pragma unroll
                    for (int m = 32; m >= 1; m >>= 1) s += __shfl_xor(s, m);
                    z[i] = s;
                }
                float zz = lane == 0 ? z[0] : lane == 1 ? z[1] : lane == 2 ? z[2] : z[3];
                zz = rbf(zz);
                float g = rbf(__builtin_amdgcn_rcpf(1.f + __builtin_amdgcn_exp2f(-1.4426950408889634f * zz)));
                if (lane < 4) __builtin_amdgcn_raw_buffer_store_b32(__builtin_bit_cast(u32, g), rws, (u32)(grow + lane) * 4u, WS_G, SC1);
            }
            WAIT_LGKM0();
#pragma unroll
            for (int i = 0; i < 4; ++i) {
                const u32 d0 = hc[i][0], d1 = hc[i][1], d2 = hc[i][2], d3 = hc[i][3], d4 = hn[i][0], d5 = hn[i][1];
                const u32 a01 = __builtin_amdgcn_alignbit(d1, d0, 16), a12 = __builtin_amdgcn_alignbit(d2, d1, 16), a23 = __builtin_amdgcn_alignbit(d3, d2, 16),
                          a34 = __builtin_amdgcn_alignbit(d4, d3, 16), a45 = __builtin_amdgcn_alignbit(d5, d4, 16);
                const u32 o = scr + 32 + (u32)(lane * 16 + i * 1024);
                lds_w128(o, (u32x4){d0, d1, d2, d3});
                lds_w128(o + TOEP_CP, (u32x4){a01, a12, a23, a34});
                lds_w128(o + 2 * TOEP_CP, (u32x4){d1, d2, d3, d4});
                lds_w128(o + 3 * TOEP_CP, (u32x4){a12, a23, a34, a45});
            }
            if (lane < 8) {
                const int sg = lane >> 1, half = lane & 1;
                const u32 d0 = rfl(hc[0][0]), d1 = rfl(hc[0][1]);
                u32 w2 = 0, w3 = 0;
                if (half) {
                    if (sg == 1) w3 = d0 << 16;
                    if (sg == 2) w3 = d0;
                    if (sg == 3) { w2 = d0 << 16; w3 = __builtin_amdgcn_alignbit(d1, d0, 16); }
                }
                lds_w128(scr + sg * TOEP_CP + half * 16, (u32x4){0u, 0u, w2, w3});
            }
            TS(9);
            WAIT_VM(0);
            if (ch == 0) {
                tag = make_tag(ep);
                *(volatile LAS u32x2*)lp(L_CTL + C_TAG) = (u32x2){(u32)tag, (u32)(tag >> 32)};
            } else {
                LDS_WAIT(ctl_get(C_TREADY) >= 1);
                tag = lds_tag();
            }
            set_flag(a.ws, WS_F0 + (u32)((xcd * 32 + l) * 2 + ch) * 8u, tag, lane);
            ctl_set(C_TREADY + ch * 4, 1);
            TS(1);
            LDS_WAIT(ctl_min2(C_LANDED) >= 8);
            poll_flags(a.ws, WS_F0 + (u32)xcd * 512u + (u32)lane * 8u, tag);
            TS(2);
            u32x4 uu[4];
#pragma unroll
            for (int i = 0; i < 4; ++i) uu[i] = __builtin_amdgcn_raw_buffer_load_b128(rws, (u32)((lane >> 2) + 16 * i) * 32768u + (u32)(lane & 3) * 16u, WS_KV + (u32)dch * 64u, SC1);
            LDS_WAIT(ctl_min4(C_DONE) >= 6);
            const u32 ub = L_U + (u32)ch * U_CH;
#pragma unroll
            for (int i = 0; i < 4; ++i) lds_w128(ub + 480 + lane * 16 + i * 1024, uu[i]);
            if (lane < 30) lds_w128(ub + lane * 16, (u32x4){0u, 0u, 0u, 0u});
            ctl_set(C_TREADY + ch * 4, 2);
            TS(3);
            LDS_WAIT(ctl_min4(C_DONE) >= 8);
        }
        __builtin_amdgcn_s_setprio(0);
        TS(4);
        const float Dv = bflo(a.ssmD[dch]);
        if (lw < 2) conv_prog<1>(ch, lane, dch, Dv, rws);
        else conv_prog<0>(ch, lane, dch, Dv, rws);
        WAIT_LGKM0();
        ctl_set(C_CDONE + lw * 4, 1);
        TS(5);
        LDS_WAIT(ctl_min4(C_CDONE) >= 1);
#pragma unroll
        for (int j = NA; j < NB; ++j) wd_dma(rwd, j, lw, wdv, wdcol);
        WAIT_VM(NB - NA);
        set_flag(a.ws, WS_F2 + (u32)(((xcd * 2 + (lw < 2 ? 1 : 0)) * 32 + l) * 2 + ch) * 8u, tag, lane);
        WAIT_VM(0);
        ctl_set(C_WDB + lw * 4, 1);
        TS(6);
        if (bid == 0 && lw == 3) {
            const GAS u64* p = (const GAS u64*)(a.ws + WS_F0) + lane * 2;
#pragma unroll 1
            for (u32 sp = 0; sp < SPIN_CAP; ++sp) {
                u64 x0 = __hip_atomic_load(p, RLX_AGENT), x1 = __hip_atomic_load(p + 128, RLX_AGENT), x2 = __hip_atomic_load(p + 256, RLX_AGENT), x3 = __hip_atomic_load(p + 384, RLX_AGENT);
                if (__all(x0 == tag && x1 == tag && x2 == tag && x3 == tag)) break;
                __builtin_amdgcn_s_sleep(16);
            }
            set_flag(a.ws, WS_EPOCH, tag, lane);
        }
        const int esw = lw & 1, edt = lw >> 1, r32 = lane & 31, hh = lane >> 5;
        const u32 qo = (u32)((row2 + 32 * esw + r32) * 1024 + c2 * 128 + edt * 64 + hh * 32);
        const u32x4 q0 = __builtin_amdgcn_raw_buffer_load_b128(rq, qo, 0, 0), q1 = __builtin_amdgcn_raw_buffer_load_b128(rq, qo + 16, 0, 0);
        LDS_WAIT(ctl_min4(C_G2) >= 1);
#pragma unroll
        for (int j = NB; j < NSTEP; ++j) wd_dma(rwd, j, lw, wdv, wdcol);
        WAIT_VM(0);
        ctl_set(C_WDC + lw * 4, 1);
        TS(7);
        {
            const int t = row2 >> 8, prog = (t == 7 || t == 6 || t == 2) ? 0 : 1;
            poll_flags(a.ws, WS_F2 + (u32)(c2 * 2 + prog) * 512u + (u32)lane * 8u, tag);
            TS(8);
            const u32 yo = WS_Y + (u32)(c2 * 64 + edt * 32) * 4096u + (u32)(row2 + 32 * esw) * 2u;
            const u32x4 y0 = __builtin_amdgcn_raw_buffer_load_b128(rws, (u32)(lane >> 2) * 4096u + (u32)(lane & 3) * 16u, yo, SC1);
            const u32x4 y1 = __builtin_amdgcn_raw_buffer_load_b128(rws, (u32)((lane >> 2) + 16) * 4096u + (u32)(lane & 3) * 16u, yo, SC1);
            const float gval = __builtin_bit_cast(float, __builtin_amdgcn_raw_buffer_load_b32(rws, (u32)(row2 + 32 * esw + r32) * 4u, WS_G, SC1));
            const u32 ys = L_YST + (u32)lw * 2048;
            lds_w128(ys + lane * 16, y0);
            lds_w128(ys + 1024 + lane * 16, y1);
            CFENCE();
            const int q4 = (lane & 15) >> 2, p4 = lane & 3, g1 = (lane >> 4) & 1;
            u32 go[8];
#pragma unroll
            for (int i = 0; i < 4; ++i) {
                u32x2 yv = lds_tr(ys + (u32)(16 * hh + 4 * i + q4) * 64 + g1 * 32 + p4 * 8);
                const u32 qa = i < 2 ? q0[2 * i] : q1[2 * i - 4], qb = i < 2 ? q0[2 * i + 1] : q1[2 * i - 3];
                go[2 * i] = pk2(gval * rbf(bflo(qa) * bflo(yv[0])), gval * rbf(bfhi(qa) * bfhi(yv[0])));
                go[2 * i + 1] = pk2(gval * rbf(bflo(qb) * bflo(yv[1])), gval * rbf(bfhi(qb) * bfhi(yv[1])));
            }
            lds_w128(L_GA + (u32)lw * 2048 + lane * 16, (u32x4){go[0], go[1], go[2], go[3]});
            lds_w128(L_GA + (u32)lw * 2048 + 1024 + lane * 16, (u32x4){go[4], go[5], go[6], go[7]});
        }
        asm volatile("s_waitcnt lgkmcnt(0)\n\ts_barrier" ::: "memory");
    }
}

extern "C" void kernel_launch(void* const* d_in, const int* in_sizes, int n_in,
                              void* d_out, int out_size, void* d_ws, size_t ws_size,
                              hipStream_t stream) {
    Args a;
    a.q = (const u16*)d_in[0]; a.k = (const u16*)d_in[1]; a.v = (const u16*)d_in[2];
    a.sk = (const u16*)d_in[3]; a.ssmk = (const u16*)d_in[4]; a.ssmD = (const u16*)d_in[5];
    a.wg = (const u16*)d_in[7]; a.wu = (const u16*)d_in[8]; a.wd = (const u16*)d_in[9];
    a.gw = (const u16*)d_in[10];
    a.out = (u16*)d_out; a.ws = (char*)d_ws;
    fused_kernel<<<256, 512, 0, stream>>>(a);
}
```

```cpp
#define PA 0
#define PB 0
#include <hip/hip_runtime.h>
#include <stdint.h>

typedef unsigned short u16;
typedef unsigned int u32;
typedef unsigned long long u64;
typedef short s16x4 __attribute__((ext_vector_type(4)));
typedef __bf16 bf16x8 __attribute__((ext_vector_type(8)));
typedef __bf16 bf16x2 __attribute__((ext_vector_type(2)));
typedef float f32x2 __attribute__((ext_vector_type(2)));
typedef float f32x4 __attribute__((ext_vector_type(4)));
typedef float f32x16 __attribute__((ext_vector_type(16)));
typedef u32 u32x2 __attribute__((ext_vector_type(2)));
typedef u32 u32x4 __attribute__((ext_vector_type(4)));
#define LAS __attribute__((address_space(3)))
#define GAS __attribute__((address_space(1)))
#define DI __device__ __forceinline__
#define RLX_AGENT __ATOMIC_RELAXED, __HIP_MEMORY_SCOPE_AGENT
#define SC1 16

constexpr int S = 2048, D = 512, F = 1376;

constexpr u32 WS_EPOCH = 0;
constexpr u32 WS_F0 = 4096;
constexpr u32 WS_F1 = 8192;
constexpr u32 WS_F2 = 16384;
constexpr u32 WS_G = 24576;
constexpr u32 WS_KV = 1u << 20;
constexpr u32 WS_Y = 3u << 20;
constexpr u32 WS_H = 5u << 20;
constexpr u32 WS_END = WS_H + 64 * 86 * 1024;
constexpr u32 WS_DBG = 16u << 20;

constexpr u32 L_X = 0;
constexpr u32 L_WG = 49152;
constexpr u32 L_WU = 86016 + 32;
constexpr u32 L_TOEP = 122912;
constexpr u32 TOEP_CP = 4160;
constexpr u32 L_SPARE = L_TOEP + 8 * TOEP_CP;
constexpr u32 L_CTL = L_SPARE + 4096;
constexpr u32 L_U = L_WG + 2 * 12288;
constexpr u32 U_CH = 4608;
constexpr u32 LDS_BYTES = 163840;
constexpr u32 C_LANDED = 0;
constexpr u32 C_DONE = 16;
constexpr u32 C_TREADY = 32;
constexpr u32 C_CDONE = 48;
constexpr u32 C_WDA = 64;
constexpr u32 C_WDB = 80;
constexpr u32 C_WDC = 96;
constexpr u32 C_G2 = 112;
constexpr u32 C_HRDY = 128;
constexpr u32 C_S0 = 160;
constexpr u32 C_TAG = 144;

__host__ __device__ constexpr u32 slot_addr(int j) {
    if (j < 4) return L_X + 2 * 16384 + j * 4096;
    if (j < 7) return L_WU + 2 * 12288 + (j - 4) * 4096;
    if (j < 8) return L_SPARE;
    if (j < 12) return L_X + (j - 8) * 4096;
    if (j < 15) return L_WG + (j - 12) * 4096;
    if (j < 18) return L_WU + (j - 15) * 4096;
    if (j < 22) return L_X + 16384 + (j - 18) * 4096;
    if (j < 25) return L_WG + 12288 + (j - 22) * 4096;
    if (j < 28) return L_WU + 12288 + (j - 25) * 4096;
    if (j < 31) return L_U + (j - 28) * 4096;
    if (j < 39) return L_TOEP + (j - 31) * 4096;
    return L_X + 2 * 16384 + (j - 39) * 4096;
}
constexpr int NA = 28, NB = 39, NSTEP = 43;
constexpr int G2_MARK = 22;
constexpr u32 L_YST = L_X;
constexpr u32 L_GA = L_X + 8192;
constexpr u32 L_EXCH = L_X + 16384;

#define MPRIO 0
#define LPRIO 1
#define HPDELAY 0
#define TOUCH 0
constexpr u32 OOB = 0x7ffffff0u;
constexpr u32 SPIN_CAP = 1u << 16;

struct Args {
    const u16 *q, *k, *v, *sk, *ssmk, *ssmD, *wg, *wu, *wd, *gw;
    u16* out;
    char* ws;
};

#define TS(i) do { if ((i) == PA) pa = __builtin_amdgcn_s_memrealtime(); if ((i) == PB) pb = __builtin_amdgcn_s_memrealtime(); } while (0)
#define ACC_BEGIN() do { } while (0)
#define ACC(i) do { } while (0)

DI float bflo(u32 x) { return __uint_as_float(x << 16); }
DI float bfhi(u32 x) { return __uint_as_float(x & 0xffff0000u); }
DI u32 pk2(float a, float b) { f32x2 p = {a, b}; return __builtin_bit_cast(u32, __builtin_convertvector(p, bf16x2)); }
DI float rbf(float a) { return bflo(pk2(a, a)); }
DI u32 rfl(u32 x) { return __builtin_amdgcn_readfirstlane(x); }
DI LAS char* lp(u32 off) { return (LAS char*)(uintptr_t)off; }
DI u32x4 lds_r128(u32 a) { return *(LAS u32x4*)lp(a); }
DI u32x2 lds_r64(u32 a) { return *(LAS u32x2*)lp(a); }
DI void lds_w128(u32 a, u32x4 v) { *(LAS u32x4*)lp(a) = v; }
DI u32x2 lds_tr(u32 a) { return __builtin_bit_cast(u32x2, __builtin_amdgcn_ds_read_tr16_b64_v4i16((LAS s16x4*)lp(a))); }
DI bf16x8 cat(u32x2 a, u32x2 b) { u32x4 v = {a[0], a[1], b[0], b[1]}; return __builtin_bit_cast(bf16x8, v); }
DI bf16x8 asb(u32x4 v) { return __builtin_bit_cast(bf16x8, v); }
#define CFENCE() asm volatile("" ::: "memory")
#define WAIT_VM(n) asm volatile("s_waitcnt vmcnt(%0)" ::"i"(n) : "memory")
#define WAIT_LGKM0() asm volatile("s_waitcnt lgkmcnt(0)" ::: "memory")
DI void ctl_set(u32 off, u32 v) { CFENCE(); *(volatile LAS u32*)lp(L_CTL + off) = v; CFENCE(); }
DI u32 ctl_min2(u32 off) { u32x2 v = *(volatile LAS u32x2*)lp(L_CTL + off); return min(rfl(v[0]), rfl(v[1])); }
DI u32 ctl_min4(u32 off) { u32x4 v = *(volatile LAS u32x4*)lp(L_CTL + off); return min(min(rfl(v[0]), rfl(v[1])), min(rfl(v[2]), rfl(v[3]))); }
DI u32 ctl_get(u32 off) { return rfl(*(volatile LAS u32*)lp(L_CTL + off)); }
#define LDS_WAIT(expr_ge) do { u32 _sp = 0; while (!(expr_ge)) { __builtin_amdgcn_s_sleep(1); if (++_sp > SPIN_CAP) break; } CFENCE(); } while (0)

DI void poll_flags(const char* ws, u32 byte_off, u64 tag) {
    const GAS u64* p = (const GAS u64*)(ws + byte_off);
#pragma unroll 1
    for (u32 sp = 0; sp < SPIN_CAP; ++sp) {
        u64 x = __hip_atomic_load(p, RLX_AGENT);
        if (__all(x == tag)) break;
        __builtin_amdgcn_s_sleep(16);
    }
    CFENCE();
}
DI void set_flag(char* ws, u32 byte_off, u64 tag, int lane) {
    if (lane == 0) __hip_atomic_store((GAS u64*)(ws + byte_off), tag, RLX_AGENT);
}
DI u64 load_epoch(const char* ws) { return __hip_atomic_load((const GAS u64*)(ws + WS_EPOCH), RLX_AGENT); }
DI u64 make_tag(u64 e) {
    u32 lo = rfl((u32)e), hi = rfl((u32)(e >> 32));
    return (((u64)hi << 32) | lo) + 0x9E3779B97F4A7C15ull;
}
DI u64 lds_tag() { u32x2 v = *(volatile LAS u32x2*)lp(L_CTL + C_TAG); return ((u64)rfl(v[1]) << 32) | rfl(v[0]); }
typedef __amdgpu_buffer_rsrc_t rsrc_t;
DI rsrc_t mk_rsrc(const void* p, u32 bytes) { return __builtin_amdgcn_make_buffer_rsrc((void*)p, 0, bytes, 0x00020000); }
DI void touch(rsrc_t r, u32 voff, u32 soff) { __builtin_amdgcn_raw_ptr_buffer_load_lds(r, (LAS void*)lp(L_CTL + 1024), 4, voff, soff, 0, 0); }
DI void dma16(rsrc_t r, u32 lds, u32 voff, u32 soff) { __builtin_amdgcn_raw_ptr_buffer_load_lds(r, (LAS void*)lp(lds), 16, voff, soff, 0, 0); }
#define SGB(mask, n) __builtin_amdgcn_sched_group_barrier(mask, n, 0)
#define SB0() __builtin_amdgcn_sched_barrier(0)
#define MFMA32(a, b, c) __builtin_amdgcn_mfma_f32_32x32x16_bf16(a, b, c, 0, 0, 0)
#define MFMA16(a, b, c) __builtin_amdgcn_mfma_f32_16x16x32_bf16(a, b, c, 0, 0, 0)

DI void wd_dma(rsrc_t rwd, int j, int i, u32 voff, u32 col_bytes) {
    const int kh = i >> 1, half = i & 1;
    dma16(rwd, slot_addr(j) + i * 1024, voff, (u32)((kh * NSTEP + j) * 16 + half * 8) * 1024u + col_bytes);
}

template <int PROG>
DI void conv_prog(int ch, int lane, int d, float Dv, rsrc_t rws) {
    constexpr int NP = PROG == 0 ? 8 : 6;
    constexpr int NT = PROG == 0 ? 3 : 5;
    constexpr int TL[5] = {PROG == 0 ? 7 : 5, PROG == 0 ? 6 : 4, PROG == 0 ? 2 : 3, 1, 0};
    const int r = lane & 15, kg = lane >> 4;
    const int w0 = 9 + r - 8 * kg;
    const int sg = (w0 + 16) & 3;
    u32 aaddr = L_TOEP + (u32)(ch * 4 + sg) * TOEP_CP + (u32)(w0 + 16 - sg) * 2;
    u32 baddr = L_U + (u32)ch * U_CH + 480 + (u32)(8 * kg + 16 * r - 16) * 2;
    f32x4 acc[NT];
#pragma unroll
    for (int i = 0; i < NT; ++i) acc[i] = (f32x4){0.f, 0.f, 0.f, 0.f};
    bf16x8 A[2][NP], B[2][NP];
    auto ld = [&](int buf) {
#pragma unroll
        for (int p = 0; p < NP; ++p) {
            u32x2 lo = *(volatile LAS u32x2*)lp(aaddr + p * 512), hi = *(volatile LAS u32x2*)lp(aaddr + p * 512 + 8);
            A[buf][p] = cat(lo, hi);
            B[buf][NP - 1 - p] = asb(lds_r128(baddr + (NP - 1 - p) * 512));
        }
        aaddr += 64;
        baddr -= 64;
        asm volatile("" : "+v"(aaddr), "+v"(baddr));
    };
    auto mm = [&](int buf) {
#pragma unroll
        for (int p = 0; p < NP; ++p)
#pragma unroll
            for (int i = 0; i < NT; ++i)
                if (p <= TL[i]) acc[i] = MFMA16(A[buf][p], B[buf][TL[i] - p], acc[i]);
    };
    auto pat = [&]() {
#pragma unroll
        for (int i = 0; i < NP; ++i) { SGB(0x8, 1); SGB(0x100, 2); SGB(0x8, 1); SGB(0x100, 1); }
        SGB(0x8, 18 - 2 * NP);
    };
    ld(0);
    SB0();
#pragma unroll 1
    for (int rho = 0; rho < 8; rho += 2) {
        ld(1); mm(0); pat(); SB0();
        ld(0); mm(1); pat(); SB0();
    }
    const u32 uaddr = L_U + (u32)ch * U_CH + 480 + (u32)(16 * r + 8 * (kg >> 1) + 4 * (1 - (kg & 1))) * 2;
#pragma unroll
    for (int i = 0; i < NT; ++i) {
        u32x2 u = lds_r64(uaddr + TL[i] * 512);
        float y0 = rbf(acc[i][0]) + rbf(bfhi(u[1]) * Dv);
        float y1 = rbf(acc[i][1]) + rbf(bflo(u[1]) * Dv);
        float y2 = rbf(acc[i][2]) + rbf(bfhi(u[0]) * Dv);
        float y3 = rbf(acc[i][3]) + rbf(bflo(u[0]) * Dv);
        u32x2 o = {pk2(y0, y1), pk2(y2, y3)};
        __builtin_amdgcn_raw_buffer_store_b64(o, rws, (u32)(32 * r + 8 * kg), WS_Y + (u32)d * 4096u + TL[i] * 512, SC1);
    }
}

__global__ __launch_bounds__(512, 2) void fused_kernel(Args a) {
    __shared__ __attribute__((aligned(128))) char smem[LDS_BYTES];
    const int tid = threadIdx.x, lane = tid & 63;
    const int wave = rfl(tid >> 6);
    const int bid = blockIdx.x, xcd = bid & 7, l = bid >> 3;
    u64 pa = 0, pb = 0;
    TS(0);
    if (tid < 48) ((volatile LAS u32*)((LAS char*)smem + L_CTL))[tid] = 0;

    const int rt = (xcd & 1) * 8 + (l & 7), cq = xcd >> 1, cj = l >> 3, ct = cq * 4 + cj;
    const int unit0 = 11 * cq + 3 * cj;
    const int nunits = min(3, min(11 * cq + 11, 43) - unit0);
    const int row2 = (xcd & 1) * 1024 + (xcd >> 1) * 256 + (l & 3) * 64, c2 = l >> 2;

    const rsrc_t rws = mk_rsrc(a.ws, WS_END);
    const rsrc_t rq = mk_rsrc(a.q, S * D * 2);
    const rsrc_t rwd = mk_rsrc(a.wd, F * D * 2);

    const u32 xv0 = (u32)(lane >> 3) * 1024 + (u32)(((lane & 7) ^ (lane >> 4)) * 16);
    const u32 xv1 = xv0 ^ 64;
    u32 wv[3];
#pragma unroll
    for (int m = 0; m < 3; ++m) {
        int n = 64 * m + lane, rr = n / 12, c = n - rr * 12;
        wv[m] = c < 4 * nunits ? (u32)(rr * (F * 2) + c * 16) : OOB;
    }
    const rsrc_t rw = mk_rsrc((wave & 1) ? a.wu : a.wg, F * D * 2);
    const u32 lwb = (wave & 1) ? L_WU : L_WG, ws0 = (u32)unit0 * 64;
    asm volatile("s_waitcnt lgkmcnt(0)\n\ts_barrier" ::: "memory");
    if (wave < 4) {
        __builtin_amdgcn_s_setprio(MPRIO);
        {
            const u32 xs = (u32)(rt * 128 + wave * 32) * 1024, wq = (u32)(wave >> 1);
#pragma unroll
            for (int i = 0; i < 4; ++i) dma16(rq, L_X + wave * 4096 + i * 1024, (i & 1) ? xv1 : xv0, xs + i * 8192);
#pragma unroll
            for (int i = 0; i < 6; ++i) dma16(rw, lwb + wq * 6144 + i * 1024, wv[i % 3], ws0 + (u32)(wq * 32 + (i / 3) * 16) * (F * 2));
        }
        const int sw = wave & 1, fw = wave >> 1;
        const int r32 = lane & 31, hh = lane >> 5;
        f32x16 acc[2][3];
#pragma unroll
        for (int i = 0; i < 2; ++i)
#pragma unroll
            for (int j = 0; j < 3; ++j)
#pragma unroll
                for (int e = 0; e < 16; ++e) acc[i][j][e] = 0.f;
        const u32 xa0 = L_X + (u32)(64 * sw + r32) * 128 + (u32)((hh ^ ((r32 >> 1) & 7)) * 16);
        const u32 wa0 = ((lane >> 4) & 1 ? L_WU : L_WG) + (u32)(8 * hh + ((lane & 15) >> 2)) * 192 + (u32)(3 * fw * 32 + (lane & 3) * 8);
        bf16x8 xb[2][2], wb[2][3];
        auto ldfrag = [&](int buf, u32 xa, u32 wa, int kk) {
#pragma unroll
            for (int i = 0; i < 2; ++i) xb[buf][i] = asb(lds_r128((xa ^ (u32)(kk << 5)) + i * 4096));
#pragma unroll
            for (int j = 0; j < 3; ++j) wb[buf][j] = cat(lds_tr(wa + kk * 16 * 192 + j * 32), lds_tr(wa + kk * 16 * 192 + 4 * 192 + j * 32));
        };
        auto mm = [&](int buf) {
#pragma unroll
            for (int j = 0; j < 3; ++j)
#pragma unroll
                for (int i = 0; i < 2; ++i) acc[i][j] = MFMA32(wb[buf][j], xb[buf][i], acc[i][j]);
        };
#pragma unroll
        for (int i = 0; i < 2; ++i)
#pragma unroll
            for (int j = 0; j < 3; ++j) asm volatile("" : "+v"(acc[i][j]));
        WAIT_VM(0);
        ctl_set(C_S0 + wave * 4, 1);
        LDS_WAIT(ctl_min4(C_S0) >= 1);
        TS(1);
        WAIT_LGKM0();
        u32 xa = xa0, wa = wa0;
        int b = 0;
        auto pat = [&]() { SGB(0x8, 1); SGB(0x100, 2); SGB(0x8, 1); SGB(0x100, 2); SGB(0x8, 1); SGB(0x100, 2); SGB(0x8, 1); SGB(0x100, 2); SGB(0x8, 2); };
        ldfrag(0, xa, wa, 0);
        SB0();
#pragma unroll 1
        for (int s = 0; s < 8; ++s) {
            ldfrag(1, xa, wa, 1); mm(0); pat(); SB0();
            ldfrag(0, xa, wa, 2); mm(1); pat(); SB0();
            u32x2 lv = *(volatile LAS u32x2*)lp(L_CTL + C_LANDED);
            SB0();
            ldfrag(1, xa, wa, 3);
            *(volatile LAS u32*)lp(L_CTL + C_DONE + wave * 4) = s + 1;
            mm(0);
            SGB(0x8, 1); SGB(0x100, 2); SGB(0x8, 1); SGB(0x100, 2); SGB(0x8, 1); SGB(0x100, 2); SGB(0x8, 1); SGB(0x100, 2); SGB(0x8, 1); SGB(0x200, 1); SGB(0x8, 1);
            SB0();
            b = b == 2 ? 0 : b + 1;
            xa = xa0 + b * 16384; wa = wa0 + b * 12288;
            const u32 need = min(s + 2, 8);
            if (min(rfl(lv[0]), rfl(lv[1])) < need) { ACC_BEGIN(); LDS_WAIT(ctl_min2(C_LANDED) >= need); ACC(0); }
            CFENCE();
            ldfrag(0, xa, wa, 0);
            mm(1); pat(); SB0();
        }
        TS(2);
        __builtin_amdgcn_s_setprio(2);
        {
            const float c0 = -2.f * 0.7978845608028654f * 1.4426950408889634f, c1 = c0 * 0.044715f;
#pragma unroll
            for (int j = 0; j < 3; ++j) {
                const int ftl = 3 * fw + j;
                if (ftl < 2 * nunits) {
#pragma unroll
                    for (int i = 0; i < 2; ++i) {
                        u32x4 o;
#pragma unroll
                        for (int e = 0; e < 4; ++e) {
                            const f32x2 g = {acc[i][j][2 * e], acc[i][j][2 * e + 1]}, u = {acc[i][j][8 + 2 * e], acc[i][j][9 + 2 * e]};
                            const f32x2 z = g * __builtin_elementwise_fma(g * g, (f32x2){c1, c1}, (f32x2){c0, c0});
                            const f32x2 dn = (f32x2){__builtin_amdgcn_exp2f(z[0]), __builtin_amdgcn_exp2f(z[1])} + (f32x2){1.f, 1.f};
                            const f32x2 hv = (g * u) * (f32x2){__builtin_amdgcn_rcpf(dn[0]), __builtin_amdgcn_rcpf(dn[1])};
                            o[e] = pk2(hv[0], hv[1]);
                        }
                        const int st = rt * 4 + sw * 2 + i, ft = unit0 * 2 + ftl;
                        __builtin_amdgcn_raw_buffer_store_b128(o, rws, (u32)lane * 16u + WS_H + (u32)(st * 86 + ft) * 1024u, 0, SC1);
                    }
                }
            }
        }
        TS(3);
        LDS_WAIT(ctl_min2(C_TREADY) >= 1);
        const u64 tag = lds_tag();
        WAIT_VM(0);
        set_flag(a.ws, WS_F1 + (u32)(((rt * 2 + sw) * 16 + ct) * 2 + fw) * 8u, tag, lane);
        TS(4);
        const int kh = wave >> 1;
        if (wave == 0) {
            poll_flags(a.ws, WS_F1 + (u32)(row2 >> 6) * 256u + (u32)(lane & 31) * 8u, tag);
            ctl_set(C_HRDY, 1);
        } else {
            LDS_WAIT(ctl_get(C_HRDY) >= 1);
        }
        TS(5);
        constexpr int HWIN = 16;
        u32x4 hb[NSTEP];
        const u32 hbase = WS_H + (u32)(((row2 >> 5) + sw) * 86 + kh * NSTEP) * 1024u;
#define HLOAD(j) hb[j] = __builtin_amdgcn_raw_buffer_load_b128(rws, (u32)lane * 16u + (j) * 1024, hbase, SC1)
#pragma unroll
        for (int j = 0; j < HWIN; ++j) HLOAD(j);
        const float gval = __builtin_bit_cast(float, __builtin_amdgcn_raw_buffer_load_b32(rws, (u32)(row2 + 32 * sw + r32) * 4u, WS_G, SC1));
        f32x16 c2a[2];
#pragma unroll
        for (int i = 0; i < 2; ++i)
#pragma unroll
            for (int e = 0; e < 16; ++e) c2a[i][e] = 0.f;
        const int q4 = (lane & 15) >> 2, p4 = lane & 3, g1 = (lane >> 4) & 1;
        const int drel = 16 * (p4 & 1) + 4 * (p4 >> 1) + 8 * g1;
        const u32 swz = (u32)((q4 >> 1) & 1) << 6;
        const u32 da0 = (u32)kh * 2048 + (u32)(4 * hh + q4) * 128 + (((u32)drel * 2) ^ swz);
        const u32 da1 = da0 ^ 64;
        bf16x8 a0[2], a1[2];
#define ALOAD(j) do { a0[(j) & 1] = cat(lds_tr(da0 + slot_addr(j)), lds_tr(da0 + slot_addr(j) + 1024)); \
                      a1[(j) & 1] = cat(lds_tr(da1 + slot_addr(j)), lds_tr(da1 + slot_addr(j) + 1024)); } while (0)
#define G2_RANGE(j0, j1, LASTLD) _Pragma("unroll") for (int j = j0; j < j1; ++j) { \
            if (j + HWIN < NSTEP) HLOAD(j + HWIN); \
            if (j + 1 < j1 || LASTLD) ALOAD(j + 1); \
            c2a[0] = MFMA32(a0[j & 1], asb(hb[j]), c2a[0]); \
            c2a[1] = MFMA32(a1[j & 1], asb(hb[j]), c2a[1]); \
            SGB(0x8, 1); SGB(0x100, 2); SGB(0x20, 1); SGB(0x8, 1); SGB(0x100, 2); SB0(); }
        LDS_WAIT(ctl_min2(C_WDA) >= 1);
        ALOAD(0);
        SB0();
        G2_RANGE(0, G2_MARK, true)
        ctl_set(C_G2 + wave * 4, 1);
        G2_RANGE(G2_MARK, NA - 1, true)
        LDS_WAIT(ctl_min4(C_WDB) >= 1);
        G2_RANGE(NA - 1, NB - 1, true)
        LDS_WAIT(ctl_min4(C_WDC) >= 1);
        G2_RANGE(NB - 1, NSTEP, false)
        TS(6);
        {
            LDS_WAIT(ctl_min4(C_G2) >= 1);
            const f32x16 snd = kh ? c2a[0] : c2a[1];
            f32x16 keep = kh ? c2a[1] : c2a[0];
#pragma unroll
            for (int e = 0; e < 4; ++e) {
                f32x4 t = {snd[4 * e], snd[4 * e + 1], snd[4 * e + 2], snd[4 * e + 3]};
                lds_w128(L_EXCH + (u32)wave * 4096 + e * 1024 + lane * 16, __builtin_bit_cast(u32x4, t));
            }
            asm volatile("s_waitcnt lgkmcnt(0)\n\ts_barrier" ::: "memory");
            TS(7);
            const float omg = rbf(1.f - gval);
            u32x4 ga0 = lds_r128(L_GA + (u32)wave * 2048 + lane * 16), ga1 = lds_r128(L_GA + (u32)wave * 2048 + 1024 + lane * 16);
            u32 o[8];
#pragma unroll
            for (int e = 0; e < 4; ++e) {
                f32x4 t = __builtin_bit_cast(f32x4, lds_r128(L_EXCH + (u32)(wave ^ 2) * 4096 + e * 1024 + lane * 16));
                u32 gA = e < 2 ? ga0[2 * e] : ga1[2 * e - 4], gB = e < 2 ? ga0[2 * e + 1] : ga1[2 * e - 3];
                o[2 * e] = pk2(__builtin_fmaf(omg, keep[4 * e] + t[0], bflo(gA)), __builtin_fmaf(omg, keep[4 * e + 1] + t[1], bfhi(gA)));
                o[2 * e + 1] = pk2(__builtin_fmaf(omg, keep[4 * e + 2] + t[2], bflo(gB)), __builtin_fmaf(omg, keep[4 * e + 3] + t[3], bfhi(gB)));
            }
            GAS u32x4* op = (GAS u32x4*)(a.out + (size_t)(row2 + 32 * sw + r32) * D + c2 * 64 + 32 * kh + 16 * hh);
            op[0] = (u32x4){o[0], o[1], o[2], o[3]};
            op[1] = (u32x4){o[4], o[5], o[6], o[7]};
        }
        TS(8);
        if (bid == 77 && wave == 0) { const u64 until = __builtin_amdgcn_s_memrealtime() + 4 * (pb - pa); while (__builtin_amdgcn_s_memrealtime() < until) __builtin_amdgcn_s_sleep(2); }
    } else {
        __builtin_amdgcn_s_setprio(LPRIO);
        const int lw = wave - 4;
        const int ch = lw & 1;
        const int dch = xcd * 64 + 2 * l + ch;
        const u32 wdv = (u32)(lane >> 3) * 1024 + (u32)(((lane & 7) ^ (((lane >> 4) & 1) << 2)) * 16);
        const u32 wdcol = (u32)c2 * 128;
        u64 tag;
        if (lw < 2) {
            const u32 xs0 = (u32)(rt * 128 + lw * 64) * 1024;
            const u32 lx = L_X + lw * 8192;
            const u32 txv = lane < 16 ? (u32)((xcd & 1) * 1024 + l * 32 + lw * 16 + lane) * 1024u : OOB;
            u32 twv;
            { const int u = l * 14 + lane, r = u / 7, c = u - r * 7; twv = lane < 14 ? (u32)(r * (F * 2) + cq * 704 + c * 128 - (c == 6 ? 64 : 0)) : OOB; }
            const u32 tdv = (u32)((l * 2 + lw) * 172 + lane) * 128u;
            auto issue_a = [&](int s, int b) {
#pragma unroll
                for (int i = 0; i < 8; ++i) dma16(rq, lx + b * 16384 + i * 1024, (i & 1) ? xv1 : xv0, xs0 + i * 8192 + s * 128);
#pragma unroll
                for (int i = 0; i < 2; ++i) dma16(rw, lwb + b * 12288 + i * 1024, wv[i % 3], ws0 + (u32)(s * 64 + (i / 3) * 16) * (F * 2));
            };
            auto issue_b = [&](int s, int b) {
#pragma unroll
                for (int i = 2; i < 12; ++i) dma16(rw, lwb + b * 12288 + i * 1024, wv[i % 3], ws0 + (u32)(s * 64 + (i / 3) * 16) * (F * 2));
            };
            issue_a(1, 1); issue_b(1, 1);
            issue_a(2, 2);
            WAIT_VM(10);
            ctl_set(C_LANDED + lw * 4, 2);
            TS(1);
            issue_b(2, 2);
            int b = 0;
#pragma unroll 1
            for (int s = 0; s < 5; ++s) {
                ACC_BEGIN();
                LDS_WAIT(ctl_min4(C_DONE) >= (u32)(s + 1));
                ACC(1);
                issue_a(s + 3, b);
                ACC(2);
                WAIT_VM(10);
                ACC(0);
                ctl_set(C_LANDED + lw * 4, s + 3);
                issue_b(s + 3, b);
                ACC(2);
                b = b == 2 ? 0 : b + 1;
            }
            WAIT_VM(2 * TOUCH);
            ctl_set(C_LANDED + lw * 4, 8);
            TS(2);
            LDS_WAIT(ctl_min4(C_DONE) >= 6);
#pragma unroll
            for (int j = 0; j < 8; ++j) { wd_dma(rwd, j, 2 * lw, wdv, wdcol); wd_dma(rwd, j, 2 * lw + 1, wdv, wdcol); }
            LDS_WAIT(ctl_min4(C_DONE) >= 7);
#pragma unroll
            for (int j = 8; j < 18; ++j) { wd_dma(rwd, j, 2 * lw, wdv, wdcol); wd_dma(rwd, j, 2 * lw + 1, wdv, wdcol); }
            LDS_WAIT(ctl_min4(C_DONE) >= 8);
#pragma unroll
            for (int j = 18; j < NA; ++j) { wd_dma(rwd, j, 2 * lw, wdv, wdcol); wd_dma(rwd, j, 2 * lw + 1, wdv, wdcol); }
            WAIT_VM(0);
            ctl_set(C_WDA + lw * 4, 1);
            TS(3);
            LDS_WAIT(ctl_get(C_TREADY + ch * 4) >= 2);
            tag = lds_tag();
        } else {
            const rsrc_t rk = mk_rsrc(a.k, S * D * 2), rv = mk_rsrc(a.v, S * D * 2);
            const int R0 = l * 64 + ch * 32, cg = lane & 7, rq4 = lane >> 3;
            u32x4 kk[7], vv[4], sw4[4];
#pragma unroll
            for (int i = 0; i < 7; ++i) {
                int row = R0 + 4 * rq4 + i - 3;
                u32 off = row >= 0 ? (u32)(row * 1024 + xcd * 128 + cg * 16) : OOB;
                kk[i] = __builtin_amdgcn_raw_buffer_load_b128(rk, off, 0, 0);
            }
#pragma unroll
            for (int i = 0; i < 4; ++i) vv[i] = __builtin_amdgcn_raw_buffer_load_b128(rv, (u32)((R0 + 4 * rq4 + i) * 1024 + xcd * 128 + cg * 16), 0, 0);
#pragma unroll
            for (int i = 0; i < 4; ++i) sw4[i] = *(const GAS u32x4*)(a.sk + (xcd * 64 + cg * 8) * 4 + i * 8);
            u32x4 qg[4];
            const int grow = rt * 128 + ct * 8 + ch * 4;
#pragma unroll
            for (int i = 0; i < 4; ++i) qg[i] = __builtin_amdgcn_raw_buffer_load_b128(rq, (u32)lane * 16u, (u32)(grow + i) * 1024u, 0);
            const u32x4 gwv = *(const GAS u32x4*)(a.gw + lane * 8);
            const rsrc_t rh = mk_rsrc(a.ssmk + (size_t)dch * S, S * 2);
            u32x4 hc[4]; u32x2 hn[4];
#pragma unroll
            for (int i = 0; i < 4; ++i) {
                hc[i] = __builtin_amdgcn_raw_buffer_load_b128(rh, (u32)(lane * 16 + i * 1024), 0, 0);
                hn[i] = __builtin_amdgcn_raw_buffer_load_b64(rh, (u32)(lane * 16 + i * 1024 + 16), 0, 0);
            }
            const u64 ep = load_epoch(a.ws);
            WAIT_VM(0);
            TS(10);
            const u32 scr = L_TOEP + (u32)ch * 4 * TOEP_CP;
#pragma unroll
            for (int i = 0; i < 4; ++i) {
                u32x4 o;
#pragma unroll
                for (int c = 0; c < 4; ++c) {
                    float e0 = bflo(kk[i][c]) * bfhi(sw4[c][1]);
                    e0 = __builtin_fmaf(bflo(kk[i + 1][c]), bflo(sw4[c][1]), e0);
                    e0 = __builtin_fmaf(bflo(kk[i + 2][c]), bfhi(sw4[c][0]), e0);
                    e0 = __builtin_fmaf(bflo(kk[i + 3][c]), bflo(sw4[c][0]), e0);
                    float e1 = bfhi(kk[i][c]) * bfhi(sw4[c][3]);
                    e1 = __builtin_fmaf(bfhi(kk[i + 1][c]), bflo(sw4[c][3]), e1);
                    e1 = __builtin_fmaf(bfhi(kk[i + 2][c]), bfhi(sw4[c][2]), e1);
                    e1 = __builtin_fmaf(bfhi(kk[i + 3][c]), bflo(sw4[c][2]), e1);
                    o[c] = pk2(e0 * bflo(vv[i][c]), e1 * bfhi(vv[i][c]));
                }
                lds_w128(scr + (u32)(4 * rq4 + i) * 128 + cg * 16, o);
            }
            CFENCE();
            {
                const int gq = lane >> 4, q4 = (lane & 15) >> 2, p4 = lane & 3;
#pragma unroll
                for (int i = 0; i < 4; ++i) {
                    u32x2 t0 = lds_tr(scr + (u32)(8 * gq + 7 - q4) * 128 + i * 32 + p4 * 8);
                    u32x2 t1 = lds_tr(scr + (u32)(8 * gq + 3 - q4) * 128 + i * 32 + p4 * 8);
                    u32x4 o = {t0[0], t0[1], t1[0], t1[1]};
                    __builtin_amdgcn_raw_buffer_store_b128(o, rws, (u32)((lane & 15) * 64 + gq * 16) + WS_KV + (u32)((R0 >> 5) * 512 + xcd * 64 + i * 16) * 64u, 0, SC1);
                }
            }
            TS(11);
            {
                float z[4];
#pragma unroll
                for (int i = 0; i < 4; ++i) {
                    float s = 0.f;
#pragma unroll
                    for (int c = 0; c < 4; ++c) { s += rbf(bflo(qg[i][c]) * bflo(gwv[c])); s += rbf(bfhi(qg[i][c]) * bfhi(gwv[c])); }
#pragma unroll
                    for (int m = 32; m >= 1; m >>= 1) s += __shfl_xor(s, m);
                    z[i] = s;
                }
                float zz = lane == 0 ? z[0] : lane == 1 ? z[1] : lane == 2 ? z[2] : z[3];
                zz = rbf(zz);
                float g = rbf(__builtin_amdgcn_rcpf(1.f + __builtin_amdgcn_exp2f(-1.4426950408889634f * zz)));
                if (lane < 4) __builtin_amdgcn_raw_buffer_store_b32(__builtin_bit_cast(u32, g), rws, (u32)(grow + lane) * 4u, WS_G, SC1);
            }
            WAIT_LGKM0();
#pragma unroll
            for (int i = 0; i < 4; ++i) {
                const u32 d0 = hc[i][0], d1 = hc[i][1], d2 = hc[i][2], d3 = hc[i][3], d4 = hn[i][0], d5 = hn[i][1];
                const u32 a01 = __builtin_amdgcn_alignbit(d1, d0, 16), a12 = __builtin_amdgcn_alignbit(d2, d1, 16), a23 = __builtin_amdgcn_alignbit(d3, d2, 16),
                          a34 = __builtin_amdgcn_alignbit(d4, d3, 16), a45 = __builtin_amdgcn_alignbit(d5, d4, 16);
                const u32 o = scr + 32 + (u32)(lane * 16 + i * 1024);
                lds_w128(o, (u32x4){d0, d1, d2, d3});
                lds_w128(o + TOEP_CP, (u32x4){a01, a12, a23, a34});
                lds_w128(o + 2 * TOEP_CP, (u32x4){d1, d2, d3, d4});
                lds_w128(o + 3 * TOEP_CP, (u32x4){a12, a23, a34, a45});
            }
            if (lane < 8) {
                const int sg = lane >> 1, half = lane & 1;
                const u32 d0 = rfl(hc[0][0]), d1 = rfl(hc[0][1]);
                u32 w2 = 0, w3 = 0;
                if (half) {
                    if (sg == 1) w3 = d0 << 16;
                    if (sg == 2) w3 = d0;
                    if (sg == 3) { w2 = d0 << 16; w3 = __builtin_amdgcn_alignbit(d1, d0, 16); }
                }
                lds_w128(scr + sg * TOEP_CP + half * 16, (u32x4){0u, 0u, w2, w3});
            }
            TS(9);
            WAIT_VM(0);
            if (ch == 0) {
                tag = make_tag(ep);
                *(volatile LAS u32x2*)lp(L_CTL + C_TAG) = (u32x2){(u32)tag, (u32)(tag >> 32)};
            } else {
                LDS_WAIT(ctl_get(C_TREADY) >= 1);
                tag = lds_tag();
            }
            set_flag(a.ws, WS_F0 + (u32)((xcd * 32 + l) * 2 + ch) * 8u, tag, lane);
            ctl_set(C_TREADY + ch * 4, 1);
            TS(1);
            LDS_WAIT(ctl_min2(C_LANDED) >= 8);
            poll_flags(a.ws, WS_F0 + (u32)xcd * 512u + (u32)lane * 8u, tag);
            TS(2);
            u32x4 uu[4];
#pragma unroll
            for (int i = 0; i < 4; ++i) uu[i] = __builtin_amdgcn_raw_buffer_load_b128(rws, (u32)((lane >> 2) + 16 * i) * 32768u + (u32)(lane & 3) * 16u, WS_KV + (u32)dch * 64u, SC1);
            LDS_WAIT(ctl_min4(C_DONE) >= 6);
            const u32 ub = L_U + (u32)ch * U_CH;
#pragma unroll
            for (int i = 0; i < 4; ++i) lds_w128(ub + 480 + lane * 16 + i * 1024, uu[i]);
            if (lane < 30) lds_w128(ub + lane * 16, (u32x4){0u, 0u, 0u, 0u});
            ctl_set(C_TREADY + ch * 4, 2);
            TS(3);
            LDS_WAIT(ctl_min4(C_DONE) >= 8);
        }
        __builtin_amdgcn_s_setprio(0);
        TS(4);
        const float Dv = bflo(a.ssmD[dch]);
        if (lw < 2) conv_prog<1>(ch, lane, dch, Dv, rws);
        else conv_prog<0>(ch, lane, dch, Dv, rws);
        WAIT_LGKM0();
        ctl_set(C_CDONE + lw * 4, 1);
        TS(5);
        LDS_WAIT(ctl_min4(C_CDONE) >= 1);
#pragma unroll
        for (int j = NA; j < NB; ++j) wd_dma(rwd, j, lw, wdv, wdcol);
        WAIT_VM(NB - NA);
        set_flag(a.ws, WS_F2 + (u32)(((xcd * 2 + (lw < 2 ? 1 : 0)) * 32 + l) * 2 + ch) * 8u, tag, lane);
        WAIT_VM(0);
        ctl_set(C_WDB + lw * 4, 1);
        TS(6);
        if (bid == 0 && lw == 3) {
            const GAS u64* p = (const GAS u64*)(a.ws + WS_F0) + lane * 2;
#pragma unroll 1
            for (u32 sp = 0; sp < SPIN_CAP; ++sp) {
                u64 x0 = __hip_atomic_load(p, RLX_AGENT), x1 = __hip_atomic_load(p + 128, RLX_AGENT), x2 = __hip_atomic_load(p + 256, RLX_AGENT), x3 = __hip_atomic_load(p + 384, RLX_AGENT);
                if (__all(x0 == tag && x1 == tag && x2 == tag && x3 == tag)) break;
                __builtin_amdgcn_s_sleep(16);
            }
            set_flag(a.ws, WS_EPOCH, tag, lane);
        }
        const int esw = lw & 1, edt = lw >> 1, r32 = lane & 31, hh = lane >> 5;
        const u32 qo = (u32)((row2 + 32 * esw + r32) * 1024 + c2 * 128 + edt * 64 + hh * 32);
        const u32x4 q0 = __builtin_amdgcn_raw_buffer_load_b128(rq, qo, 0, 0), q1 = __builtin_amdgcn_raw_buffer_load_b128(rq, qo + 16, 0, 0);
        LDS_WAIT(ctl_min4(C_G2) >= 1);
#pragma unroll
        for (int j = NB; j < NSTEP; ++j) wd_dma(rwd, j, lw, wdv, wdcol);
        WAIT_VM(0);
        ctl_set(C_WDC + lw * 4, 1);
        TS(7);
        {
            const int t = row2 >> 8, prog = (t == 7 || t == 6 || t == 2) ? 0 : 1;
            poll_flags(a.ws, WS_F2 + (u32)(c2 * 2 + prog) * 512u + (u32)lane * 8u, tag);
            TS(8);
            const u32 yo = WS_Y + (u32)(c2 * 64 + edt * 32) * 4096u + (u32)(row2 + 32 * esw) * 2u;
            const u32x4 y0 = __builtin_amdgcn_raw_buffer_load_b128(rws, (u32)(lane >> 2) * 4096u + (u32)(lane & 3) * 16u, yo, SC1);
            const u32x4 y1 = __builtin_amdgcn_raw_buffer_load_b128(rws, (u32)((lane >> 2) + 16) * 4096u + (u32)(lane & 3) * 16u, yo, SC1);
            const float gval = __builtin_bit_cast(float, __builtin_amdgcn_raw_buffer_load_b32(rws, (u32)(row2 + 32 * esw + r32) * 4u, WS_G, SC1));
            const u32 ys = L_YST + (u32)lw * 2048;
            lds_w128(ys + lane * 16, y0);
            lds_w128(ys + 1024 + lane * 16, y1);
            CFENCE();
            const int q4 = (lane & 15) >> 2, p4 = lane & 3, g1 = (lane >> 4) & 1;
            u32 go[8];
#pragma unroll
            for (int i = 0; i < 4; ++i) {
                u32x2 yv = lds_tr(ys + (u32)(16 * hh + 4 * i + q4) * 64 + g1 * 32 + p4 * 8);
                const u32 qa = i < 2 ? q0[2 * i] : q1[2 * i - 4], qb = i < 2 ? q0[2 * i + 1] : q1[2 * i - 3];
                go[2 * i] = pk2(gval * rbf(bflo(qa) * bflo(yv[0])), gval * rbf(bfhi(qa) * bfhi(yv[0])));
                go[2 * i + 1] = pk2(gval * rbf(bflo(qb) * bflo(yv[1])), gval * rbf(bfhi(qb) * bfhi(yv[1])));
            }
            lds_w128(L_GA + (u32)lw * 2048 + lane * 16, (u32x4){go[0], go[1], go[2], go[3]});
            lds_w128(L_GA + (u32)lw * 2048 + 1024 + lane * 16, (u32x4){go[4], go[5], go[6], go[7]});
        }
        asm volatile("s_waitcnt lgkmcnt(0)\n\ts_barrier" ::: "memory");
    }
}

extern "C" void kernel_launch(void* const* d_in, const int* in_sizes, int n_in,
                              void* d_out, int out_size, void* d_ws, size_t ws_size,
                              hipStream_t stream) {
    Args a;
    a.q = (const u16*)d_in[0]; a.k = (const u16*)d_in[1]; a.v = (const u16*)d_in[2];
    a.sk = (const u16*)d_in[3]; a.ssmk = (const u16*)d_in[4]; a.ssmD = (const u16*)d_in[5];
    a.wg = (const u16*)d_in[7]; a.wu = (const u16*)d_in[8]; a.wd = (const u16*)d_in[9];
    a.gw = (const u16*)d_in[10];
    a.out = (u16*)d_out; a.ws = (char*)d_ws;
    fused_kernel<<<256, 512, 0, stream>>>(a);
}
```

```cpp
#include <hip/hip_runtime.h>
#include <stdint.h>

typedef unsigned short u16;
typedef unsigned int u32;
typedef unsigned long long u64;
typedef short s16x4 __attribute__((ext_vector_type(4)));
typedef __bf16 bf16x8 __attribute__((ext_vector_type(8)));
typedef __bf16 bf16x2 __attribute__((ext_vector_type(2)));
typedef float f32x2 __attribute__((ext_vector_type(2)));
typedef float f32x4 __attribute__((ext_vector_type(4)));
typedef float f32x16 __attribute__((ext_vector_type(16)));
typedef u32 u32x2 __attribute__((ext_vector_type(2)));
typedef u32 u32x4 __attribute__((ext_vector_type(4)));
#define LAS __attribute__((address_space(3)))
#define GAS __attribute__((address_space(1)))
#define DI __device__ __forceinline__
#define RLX_AGENT __ATOMIC_RELAXED, __HIP_MEMORY_SCOPE_AGENT
#define SC1 16

constexpr int S = 2048, D = 512, F = 1376;

constexpr u32 WS_EPOCH = 0;
constexpr u32 WS_G = 4096;
constexpr u32 FL = 128;
constexpr u32 WS_F0 = 65536;
constexpr u32 WS_F1 = 131072;
constexpr u32 WS_F2 = 262144;
constexpr u32 WS_KV = 1u << 20;
constexpr u32 WS_Y = 3u << 20;
constexpr u32 WS_H = 5u << 20;
constexpr u32 WS_END = WS_H + 64 * 86 * 1024;
constexpr u32 WS_DBG = 16u << 20;

constexpr u32 L_X = 0;
constexpr u32 L_W = 49152;
constexpr u32 WBUF = 24640, WU_OFF = 12320;
constexpr u32 L_REST = L_W + 4 * WBUF;
constexpr u32 L_CTL = L_REST + 12288;
constexpr u32 L_CONV = L_W;
constexpr u32 TOEP_CP = 4160;
constexpr u32 U_OFF = 4 * TOEP_CP;
constexpr u32 LDS_BYTES = 163840;
constexpr u32 C_LANDED = 0;
constexpr u32 C_ISS = 8;
constexpr u32 C_DONE = 16;
constexpr u32 C_HP = 32;
constexpr u32 C_WDA = 40;
constexpr u32 C_CDONE = 48;
constexpr u32 C_WDB = 64;
constexpr u32 C_G2 = 80;
constexpr u32 C_S0 = 96;
constexpr u32 C_GEG = 112;
constexpr u32 C_TAG = 128;
constexpr u32 C_TAGOK = 136;
constexpr u32 C_HRDY = 140;
constexpr u32 C_W7 = 152;
constexpr u32 C_CONVOK = 144;

__device__ __forceinline__ constexpr u32 slot_addr(int j) {
    if (j >= 27) j -= 27;
    if (j < 4) return L_X + 2 * 16384 + j * 4096;
    if (j < 8) return L_X + (j - 4) * 4096;
    if (j < 12) return L_X + 16384 + (j - 8) * 4096;
    if (j < 15) return L_REST + (j - 12) * 4096;
    if (j < 21) return L_W + 2 * WBUF + (j - 15) * 4096;
    return L_W + 3 * WBUF + (j - 21) * 4096;
}
constexpr int NA = 27, NSTEP = 43;
constexpr u32 YST_OFF = 8192;
constexpr u32 GA_OFF = 12288;
constexpr u32 L_EXCH = L_W + 2 * WBUF + 4096;

#define ROT 1
#define PSLEEP 2
#define LATER 6
#define TOUCH 0
#define WL0 5
#define WLN 5
#define LDPRIO 0
#define R2MUL 0
#define G2PRIO 1
#define CVPRIO 2
#define R2X 0
#define LVPOS 14
#define HWIN 16
constexpr u32 CODE_BYTES = 35840;
constexpr u32 OOB = 0x7ffffff0u;
constexpr u32 SPIN_CAP = 1u << 16;

struct Args {
    const u16 *q, *k, *v, *sk, *ssmk, *ssmD, *wg, *wu, *wd, *gw;
    u16* out;
    char* ws;
};

#define TS(i) do { } while (0)

DI float bflo(u32 x) { return __uint_as_float(x << 16); }
DI float bfhi(u32 x) { return __uint_as_float(x & 0xffff0000u); }
DI u32 pk2(float a, float b) { f32x2 p = {a, b}; return __builtin_bit_cast(u32, __builtin_convertvector(p, bf16x2)); }
DI float sfma(float a, float b, float c) { float r; asm("v_fma_f32 %0, %1, %2, %3" : "=v"(r) : "v"(a), "v"(b), "v"(c)); return r; }
DI float smul(float a, float b) { float r; asm("v_mul_f32 %0, %1, %2" : "=v"(r) : "v"(a), "v"(b)); return r; }
DI float sadd(float a, float b) { float r; asm("v_add_f32 %0, %1, %2" : "=v"(r) : "v"(a), "v"(b)); return r; }
DI u32 smov(u32 a) { u32 r; asm("v_mov_b32 %0, %1" : "=v"(r) : "v"(a)); return r; }
DI float rbf(float a) { return bflo(pk2(a, a)); }
DI u32 rfl(u32 x) { return __builtin_amdgcn_readfirstlane(x); }
DI LAS char* lp(u32 off) { return (LAS char*)(uintptr_t)off; }
DI u32x4 lds_r128(u32 a) { return *(LAS u32x4*)lp(a); }
DI u32x2 lds_r64(u32 a) { return *(LAS u32x2*)lp(a); }
DI void lds_w128(u32 a, u32x4 v) { *(LAS u32x4*)lp(a) = v; }
DI u32x2 lds_tr(u32 a) { return __builtin_bit_cast(u32x2, __builtin_amdgcn_ds_read_tr16_b64_v4i16((LAS s16x4*)lp(a))); }
DI bf16x8 cat(u32x2 a, u32x2 b) { u32x4 v = {a[0], a[1], b[0], b[1]}; return __builtin_bit_cast(bf16x8, v); }
DI bf16x8 asb(u32x4 v) { return __builtin_bit_cast(bf16x8, v); }
#define CFENCE() asm volatile("" ::: "memory")
#define WAIT_VM(n) asm volatile("s_waitcnt vmcnt(%0)" ::"i"(n) : "memory")
#define WAIT_LGKM0() asm volatile("s_waitcnt lgkmcnt(0)" ::: "memory")
#define BARRIER() asm volatile("s_waitcnt lgkmcnt(0)\n\ts_barrier" ::: "memory")
DI void ctl_set(u32 off, u32 v) { CFENCE(); *(volatile LAS u32*)lp(L_CTL + off) = v; CFENCE(); }
DI u32 ctl_min2(u32 off) { u32x2 v = *(volatile LAS u32x2*)lp(L_CTL + off); return rfl(min(v[0], v[1])); }
DI u32 ctl_min4(u32 off) { u32x4 v = *(volatile LAS u32x4*)lp(L_CTL + off); return rfl(min(min(v[0], v[1]), min(v[2], v[3]))); }
DI u32 ctl_get(u32 off) { return rfl(*(volatile LAS u32*)lp(L_CTL + off)); }
#define LDS_WAIT(cond) do { u32 _sp = 0; _Pragma("unroll 1") while (!(cond)) { __builtin_amdgcn_s_sleep(1); if (++_sp > SPIN_CAP * 4) break; } CFENCE(); } while (0)
#define LDS_SPIN(cond) do { u32 _sp = 0; _Pragma("unroll 1") while (!(cond)) { if (++_sp > SPIN_CAP * 4) break; } CFENCE(); } while (0)

DI void poll_flags(const char* ws, u32 byte_off, u64 tag) {
    const GAS u64* p = (const GAS u64*)(ws + byte_off);
    bool ok = false;
#pragma unroll 1
    for (u32 sp = 0; sp < SPIN_CAP; ++sp) {
        if (!ok) ok = __hip_atomic_load(p, RLX_AGENT) == tag;
        if (__all(ok)) break;
        __builtin_amdgcn_s_sleep(PSLEEP);
    }
    CFENCE();
}
DI void set_flag(char* ws, u32 byte_off, u64 tag, int lane) {
    if (lane == 0) __hip_atomic_store((GAS u64*)(ws + byte_off), tag, RLX_AGENT);
}
DI u64 lds_tag() { u32x2 v = *(volatile LAS u32x2*)lp(L_CTL + C_TAG); return ((u64)rfl(v[1]) << 32) | rfl(v[0]); }
typedef __amdgpu_buffer_rsrc_t rsrc_t;
DI rsrc_t mk_rsrc(const void* p, u32 bytes) { return __builtin_amdgcn_make_buffer_rsrc((void*)p, 0, bytes, 0x00020000); }
DI void dma16(rsrc_t r, u32 lds, u32 voff, u32 soff) { __builtin_amdgcn_raw_ptr_buffer_load_lds(r, (LAS void*)lp(lds), 16, voff, soff, 0, 0); }
#define SGB(mask, n) __builtin_amdgcn_sched_group_barrier(mask, n, 0)
#define SB0() __builtin_amdgcn_sched_barrier(0)
#define MFMA32(a, b, c) __builtin_amdgcn_mfma_f32_32x32x16_bf16(a, b, c, 0, 0, 0)
#define MFMA16(a, b, c) __builtin_amdgcn_mfma_f32_16x16x32_bf16(a, b, c, 0, 0, 0)
template <int CTRL, int RM = 0xf> DI float dpp_add(float x) { return x + __builtin_bit_cast(float, __builtin_amdgcn_update_dpp(0, __builtin_bit_cast(int, x), CTRL, RM, 0xf, true)); }

__global__ __launch_bounds__(512, 2) void fused_kernel(Args a) {
    __shared__ __attribute__((aligned(128))) char smem[LDS_BYTES];
    const int tid = threadIdx.x, lane = tid & 63;
    const int wave = rfl(tid >> 6);
    const int bid = blockIdx.x, xcd = bid & 7, l = bid >> 3;
    TS(0);
    u32 cpf = 0;
    if (wave == 7) {
        u64 pc;
        asm volatile("s_getpc_b64 %0" : "=s"(pc));
        const rsrc_t rc = mk_rsrc((const void*)(pc & ~255ull), CODE_BYTES);
        cpf = __builtin_amdgcn_raw_buffer_load_b32(rc, (u32)(((l & 7) * 64 + lane) * 128), 0, 0);
    }
    if (wave == 6) ((volatile LAS u32*)((LAS char*)smem + L_CTL))[lane] = 0;
    BARRIER();

    const int rt = (xcd & 1) * 8 + (l & 7), cq = xcd >> 1, cj = l >> 3, ct = cq * 4 + cj;
    const int unit0 = 11 * cq + 3 * cj;
    const int nunits = min(3, min(11 * cq + 11, 43) - unit0);
    const int rot = (l + cj) & ROT;
    const int row2 = (xcd & 1) * 1024 + (xcd >> 1) * 256 + (l & 3) * 64, c2 = l >> 2;

    const int r2 = ((l & 3) * R2MUL + xcd * R2X) % NSTEP;
#define KBLK(j) ((j) + r2 >= NSTEP ? (j) + r2 - NSTEP : (j) + r2)

    const rsrc_t rws = mk_rsrc(a.ws, WS_END);
    const rsrc_t rq = mk_rsrc(a.q, S * D * 2);
    const rsrc_t rwd = mk_rsrc(a.wd, F * D * 2);

    const u32 xv0 = (u32)(lane >> 3) * 1024 + (u32)(((lane & 7) ^ (lane >> 4)) * 16);
    const u32 xv1 = xv0 ^ 64;
    u32 wv[3];
#pragma unroll
    for (int m = 0; m < 3; ++m) {
        int n = 64 * m + lane, rr = n / 12, c = n - rr * 12;
        wv[m] = c < 4 * nunits ? (u32)(rr * (F * 2) + (c ^ ((rr >> 2) & 2)) * 16) : OOB;
    }
    const rsrc_t rw = mk_rsrc((wave & 1) ? a.wu : a.wg, F * D * 2);
    const u32 lwb = L_W + ((wave & 1) ? WU_OFF : 0), ws0 = (u32)unit0 * 64;

#define WDV ((u32)(lane >> 3) * 1024 + (u32)(((lane & 7) ^ (((lane >> 4) & 1) << 2)) * 16))

    if (wave < 4) {
        __builtin_amdgcn_s_setprio(2);
        TS(9);
        {
            const u32 xs = (u32)(rt * 128 + wave * 32) * 1024, wq = (u32)(wave >> 1);
#pragma unroll
            for (int i = 0; i < 4; ++i) dma16(rq, L_X + wave * 4096 + i * 1024, (i & 1) ? xv1 : xv0, xs + i * 8192 + rot * 128);
#pragma unroll
            for (int i = 0; i < 6; ++i) dma16(rw, lwb + wq * 6144 + i * 1024, wv[i % 3], ws0 + (u32)(rot * 64 + wq * 32 + (i / 3) * 16) * (F * 2));
        }
        const int sw = wave & 1, fw = wave >> 1;
        const int r32 = lane & 31, hh = lane >> 5;
        const int r16 = lane & 15, kg = lane >> 4;
        f32x4 acc[4][6];
#pragma unroll
        for (int i = 0; i < 4; ++i)
#pragma unroll
            for (int j = 0; j < 6; ++j) acc[i][j] = (f32x4){0.f, 0.f, 0.f, 0.f};
        const u32 wrow = L_W + (u32)(8 * kg + (r16 >> 2)) * 192 + (u32)((lane & 3) * 8);
        u32 wa0[2][2];
        wa0[0][0] = wrow + (((u32)(3 * fw) * 32) ^ ((kg & 1) * 32)); wa0[0][1] = wrow + (((u32)(3 * fw + 1) * 32) ^ ((kg & 1) * 32));
        wa0[1][0] = wa0[0][0] + 2 * WBUF; wa0[1][1] = wa0[0][1] + 2 * WBUF;
        asm volatile("" : "+v"(wa0[0][0]), "+v"(wa0[0][1]), "+v"(wa0[1][0]), "+v"(wa0[1][1]));
        u32 xa0 = L_X + (u32)(64 * sw + r16) * 128 + (u32)((kg ^ ((r16 >> 1) & 7)) * 16), xa1 = xa0 ^ 64;
        asm volatile("" : "+v"(xa0), "+v"(xa1));
        u32x4 xb[2][8];
        u32x2 wb[2][12];
        auto ldx1 = [&](int xs, u32 xo, int n) { xb[xs][n] = lds_r128((n >> 2 ? xa1 : xa0) + xo + (n & 3) * 2048); };
        auto ldw1 = [&](int buf, int s, int kk, int n) {
            const int j = n >> 1;
            wb[buf][n] = lds_tr(wa0[(s >> 1) & 1][(j % 3) & 1] + (s & 1) * WBUF + (j / 3) * WU_OFF + kk * 32 * 192 + ((j % 3) >> 1) * 64 + (n & 1) * 4 * 192);
        };
        auto mm1 = [&](int buf, int xs, int kk, int n) {
            const int j = n >> 2, i = n & 3;
            const u32x4 av = {wb[buf][2 * j][0], wb[buf][2 * j][1], wb[buf][2 * j + 1][0], wb[buf][2 * j + 1][1]};
            asm volatile("v_mfma_f32_16x16x32_bf16 %0, %1, %2, %0" : "+v"(acc[i][j]) : "v"(av), "v"(xb[xs][kk * 4 + i]));
        };
#pragma unroll
        for (int i = 0; i < 4; ++i)
#pragma unroll
            for (int j = 0; j < 6; ++j) asm volatile("" : "+v"(acc[i][j]));
        TS(10);
        WAIT_VM(0);
        TS(11);
        ctl_set(C_S0 + wave * 4, 1);
        LDS_SPIN(ctl_min4(C_S0) >= 1);
        TS(1);
        WAIT_LGKM0();
#pragma unroll
        for (int n = 0; n < 4; ++n) ldx1(0, 0, n);
#pragma unroll
        for (int n = 0; n < 12; ++n) ldw1(0, 0, 0, n);
#pragma unroll
        for (int n = 4; n < 8; ++n) ldx1(0, 0, n);
        *(volatile LAS u32*)lp(L_CTL + C_DONE + wave * 4) = 1;
        SB0();
        const int kh = wave >> 1;
        u32x4 wl[16];
        const u32 wdv = WDV, wds = (u32)(kh * NSTEP * 16 + sw * 8) * 1024u + (u32)c2 * 128;
        auto wload = [&](int n) { if (n >= 0 && n < 16) wl[n] = __builtin_amdgcn_raw_buffer_load_b128(rwd, wdv, wds + (u32)KBLK(NA + n) * 16384u, 0); };
        auto stage = [&](int s, int xs) {
            const u32 xa = ((s + 1) % 3) * 16384;
            volatile LAS u32* const dp = (volatile LAS u32*)lp(L_CTL + C_DONE + wave * 4);
            u32x2 lv = {0, 0};
#pragma unroll
            for (int n = 0; n < 24; ++n) {
                mm1(0, xs, 0, n);
                if (n < 12) ldw1(1, s, 1, n);
                if (s > 0 && s < 7) {
                    if (n >= 12 && n < 20) ldx1(xs ^ 1, xa, n - 12);
                    if (n == 19) *dp = s + 2;
                }
                if (s == 7 && n == 11) *dp = 10;
                if (s < 7 && n == LVPOS) lv = *(volatile LAS u32x2*)lp(L_CTL + C_LANDED);
                if (s == 7 && n >= 13 && (n & 1)) wload(5 + (n - 13) / 2);
                SB0();
            }
            if (s < 7) { if (rfl(min(lv[0], lv[1])) < (u32)(s + 2)) LDS_SPIN(ctl_min2(C_LANDED) >= (u32)(s + 2)); }
            CFENCE();
#pragma unroll
            for (int n = 0; n < 24; ++n) {
                mm1(1, xs, 1, n);
                if (s == 0) {
                    if (n < 4) ldx1(1, xa, n); else if (n < 16) ldw1(0, 1, 0, n - 4); else if (n < 20) ldx1(1, xa, n - 12);
                    if (n == 19) *dp = 2;
                } else if (s < 7) {
                    if (n % 3 != 2 && n < 18) ldw1(0, s + 1, 0, n - n / 3);
                    if (s == 5 && (n == 19 || n == 22)) wload((n - 19) / 3);
                    if (s == 6 && (n == 18 || n == 20 || n == 22)) wload(2 + (n - 18) / 2);
                } else if (n < 15 && n % 3 == 0) wload(11 + n / 3);
                SB0();
            }
        };
        stage(0, 0); stage(1, 1); stage(2, 0); stage(3, 1); stage(4, 0); stage(5, 1); stage(6, 0); stage(7, 1);
        asm volatile("s_nop 7" ::: "memory");
        SB0();
        TS(2);
        {
            const float c0 = -2.f * 0.7978845608028654f * 1.4426950408889634f, c1 = c0 * 0.044715f;
            const u32 hv0 = (u32)lane * 16u + WS_H + (u32)((rt * 4 + sw * 2) * 86 + unit0 * 2 + 3 * fw) * 1024u;
#pragma unroll
            for (int j = 0; j < 3; ++j) {
                const u32 hvo = 3 * fw + j < 2 * nunits ? hv0 + j * 1024 : OOB;
#pragma unroll
                for (int i = 0; i < 2; ++i) {
                    u32 hp[4];
#pragma unroll
                    for (int e = 0; e < 4; ++e) {
                        const f32x4 G = acc[2 * i + (e >> 1)][j], U = acc[2 * i + (e >> 1)][3 + j];
                        const f32x2 g = {G[2 * (e & 1)], G[2 * (e & 1) + 1]}, u = {U[2 * (e & 1)], U[2 * (e & 1) + 1]};
                        const f32x2 z = g * __builtin_elementwise_fma(g * g, (f32x2){c1, c1}, (f32x2){c0, c0});
                        const f32x2 dn = (f32x2){__builtin_amdgcn_exp2f(z[0]), __builtin_amdgcn_exp2f(z[1])} + (f32x2){1.f, 1.f};
                        const f32x2 hv = (g * u) * (f32x2){__builtin_amdgcn_rcpf(dn[0]), __builtin_amdgcn_rcpf(dn[1])};
                        hp[e] = pk2(hv[0], hv[1]);
                    }
                    const auto s0 = __builtin_amdgcn_permlane16_swap(hp[0], hp[2], false, false);
                    const auto s1 = __builtin_amdgcn_permlane16_swap(hp[1], hp[3], false, false);
                    const u32x4 o = {s0[0], s1[0], s0[1], s1[1]};
                    __builtin_amdgcn_raw_buffer_store_b128(o, rws, hvo + i * 86 * 1024, 0, SC1);
                }
            }
        }
        ctl_set(C_GEG + wave * 4, 1);
        __builtin_amdgcn_s_setprio(0);
        TS(3);
        LDS_WAIT(ctl_min2(C_HP) >= 1);
        const u64 tag = lds_tag();
        WAIT_VM(0);
        set_flag(a.ws, WS_F1 + (u32)(((rt * 2 + sw) * 16 + ct) * 2 + fw) * FL, tag, lane);
        TS(4);
        f32x16 c2a[2];
#pragma unroll
        for (int i = 0; i < 2; ++i)
#pragma unroll
            for (int e = 0; e < 16; ++e) c2a[i][e] = 0.f;
        const int q4 = (lane & 15) >> 2, p4 = lane & 3, g1 = (lane >> 4) & 1;
        const int drel = 16 * (p4 & 1) + 4 * (p4 >> 1) + 8 * g1;
        const u32 swz = (u32)((q4 >> 1) & 1) << 6;
        const u32 da0 = (u32)kh * 2048 + (u32)(8 * hh + q4) * 128 + (((u32)drel * 2) ^ swz);
        const u32 da1 = da0 ^ 64;
        bf16x8 a0[4], a1[4];
        constexpr u32 HI = 98304;
        u32 da0h = da0 + HI, da1h = da1 + HI;
        asm volatile("" : "+v"(da0h), "+v"(da1h));
#define SADDR(lo, hi, j) (slot_addr(j) < HI ? (lo) + slot_addr(j) : (hi) + (slot_addr(j) - HI))
#define ALOAD(j) do { a0[(j) & 3] = cat(lds_tr(SADDR(da0, da0h, j)), lds_tr(SADDR(da0, da0h, j) + 512)); \
                      a1[(j) & 3] = cat(lds_tr(SADDR(da1, da1h, j)), lds_tr(SADDR(da1, da1h, j) + 512)); } while (0)
        if (wave == 0) {
            poll_flags(a.ws, WS_F1 + (u32)((row2 >> 6) * 32 + (lane & 31)) * FL, tag);
            ctl_set(C_HRDY, 1);
        }
        LDS_WAIT(ctl_min2(C_WDA) >= 1);
        ALOAD(0); ALOAD(1); ALOAD(2);
        if (wave != 0) LDS_WAIT(ctl_get(C_HRDY) >= 1);
        __builtin_amdgcn_s_setprio(G2PRIO);
        TS(5);
        u32x4 hb[NSTEP];
        const u32 hbase = WS_H + (u32)(((row2 >> 5) + sw) * 86 + kh * NSTEP) * 1024u;
#define HLOAD(j) hb[j] = __builtin_amdgcn_raw_buffer_load_b128(rws, (u32)lane * 16u, hbase + (u32)KBLK(j) * 1024u, SC1)
#pragma unroll
        for (int j = 0; j < HWIN; ++j) { HLOAD(j); SB0(); }
        const float gval = __builtin_bit_cast(float, __builtin_amdgcn_raw_buffer_load_b32(rws, (u32)(row2 + 32 * sw + r32) * 4u, WS_G, SC1));
        WAIT_LGKM0();
        SB0();
        const u32 wla = (u32)wave * 1024 + (u32)lane * 16;
        u32 wlah = wla + HI;
        asm volatile("" : "+v"(wlah));
#define G2_RANGE(j0, j1, WOFF) _Pragma("unroll") for (int j = j0; j < j1; ++j) { \
            if (j + HWIN < NSTEP) HLOAD(j + HWIN); \
            if (j + 3 < NSTEP) ALOAD(j + 3); \
            if (WOFF >= 0) lds_w128(SADDR(wla, wlah, j - WOFF), wl[j - WOFF]); \
            c2a[0] = MFMA32(a0[j & 3], asb(hb[j]), c2a[0]); \
            c2a[1] = MFMA32(a1[j & 3], asb(hb[j]), c2a[1]); \
            SGB(0x8, 1); SGB(0x100, 2); SGB(0x20, 1); SGB(0x8, 1); SGB(0x100, 2); SGB(0x200, 1); SB0(); }
#define G2_PEEK() const u32 pk = *(volatile LAS u32*)lp(L_CTL + C_G2 + (wave ^ 1) * 4); SB0();
#define G2_NEED(n) if (rfl(pk) < n) LDS_SPIN(ctl_get(C_G2 + (wave ^ 1) * 4) >= n); CFENCE();
        G2_RANGE(0, 5, -1)
        ctl_set(C_G2 + wave * 4, 1);
        G2_RANGE(5, 6, -1)
        { G2_PEEK() G2_RANGE(6, 8, -1) G2_NEED(1) }
        G2_RANGE(8, 13, 8)
        ctl_set(C_G2 + wave * 4, 2);
        G2_RANGE(13, 14, 8)
        { G2_PEEK() G2_RANGE(14, 16, 8) G2_NEED(2) }
        ctl_set(C_G2 + wave * 4, 3);
        G2_RANGE(16, 22, 8)
        { G2_PEEK() G2_RANGE(22, 24, 8) G2_NEED(3) }
        ctl_set(C_G2 + wave * 4, 4);
        G2_RANGE(24, 30, -1)
        { G2_PEEK() G2_RANGE(30, 32, -1) G2_NEED(4) }
        G2_RANGE(32, NSTEP, -1)
        LDS_SPIN(ctl_min4(C_G2) >= 4);
        TS(6);
        {
            const f32x16 snd = kh ? c2a[0] : c2a[1];
            f32x16 keep = kh ? c2a[1] : c2a[0];
#pragma unroll
            for (int e = 0; e < 4; ++e) {
                f32x4 t = {snd[4 * e], snd[4 * e + 1], snd[4 * e + 2], snd[4 * e + 3]};
                lds_w128(L_EXCH + (u32)wave * 4096 + e * 1024 + lane * 16, __builtin_bit_cast(u32x4, t));
            }
            BARRIER();
            TS(7);
            const float omg = rbf(1.f - gval);
            const u32 gaa = L_CONV + (u32)sw * WBUF + GA_OFF + (u32)kh * 2048 + lane * 16;
            u32x4 ga0 = lds_r128(gaa), ga1 = lds_r128(gaa + 1024);
            u32 o[8];
#pragma unroll
            for (int e = 0; e < 4; ++e) {
                f32x4 t = __builtin_bit_cast(f32x4, lds_r128(L_EXCH + (u32)(wave ^ 2) * 4096 + e * 1024 + lane * 16));
                u32 gA = e < 2 ? ga0[2 * e] : ga1[2 * e - 4], gB = e < 2 ? ga0[2 * e + 1] : ga1[2 * e - 3];
                o[2 * e] = pk2(__builtin_fmaf(omg, keep[4 * e] + t[0], bflo(gA)), __builtin_fmaf(omg, keep[4 * e + 1] + t[1], bfhi(gA)));
                o[2 * e + 1] = pk2(__builtin_fmaf(omg, keep[4 * e + 2] + t[2], bflo(gB)), __builtin_fmaf(omg, keep[4 * e + 3] + t[3], bfhi(gB)));
            }
            GAS u32x4* op = (GAS u32x4*)(a.out + (size_t)(row2 + 32 * sw + r32) * D + c2 * 64 + 32 * kh + 16 * hh);
            op[0] = (u32x4){o[0], o[1], o[2], o[3]};
            op[1] = (u32x4){o[4], o[5], o[6], o[7]};
        }
        TS(8);
    } else {
        const int lw = wave - 4;
        const int ch = lw & 1;
        const int dch = xcd * 64 + 2 * l + ch;
        const u32 cb = L_CONV + (u32)ch * WBUF;
        const u32 wdv = WDV;
        if (lw < 2) {
            __builtin_amdgcn_s_setprio(LDPRIO);
            const u32 xs0 = (u32)(rt * 128 + lw * 64) * 1024;
            const u32 lx = L_X + lw * 8192;
            auto issue_x = [&](int s, int b) {
#pragma unroll
                for (int i = 0; i < 8; ++i) dma16(rq, lx + b * 16384 + i * 1024, (i & 1) ? xv1 : xv0, xs0 + i * 8192 + (s ^ rot) * 128);
            };
            auto issue_w = [&](int s) {
#pragma unroll
                for (int i = 0; i < 12; ++i) dma16(rw, lwb + (s & 3) * WBUF + i * 1024, wv[i % 3], ws0 + (u32)((s ^ rot) * 64 + (i / 3) * 16) * (F * 2));
            };
            u32 dn = 0;
#define NEED_DONE(n) do { if (dn < (u32)(n)) LDS_SPIN((dn = ctl_min4(C_DONE)) >= (u32)(n)); } while (0)
            issue_x(1, 1);
            issue_w(1);
            issue_x(2, 2);
            issue_w(2);
            WAIT_VM(12);
            ctl_set(C_LANDED + lw * 4, 2);
            TS(1);
            int b = 0;
#pragma unroll 1
            for (int n = 1; n < 6; ++n) {
                NEED_DONE(n);
                issue_x(n + 2, b);
                WAIT_VM(20);
                ctl_set(C_LANDED + lw * 4, n + 1);
                issue_w(n + 2);
                if (n == 1) { WAIT_VM(12); ctl_set(C_LANDED + lw * 4, 3); }
                b = b == 2 ? 0 : b + 1;
            }
            ctl_set(C_W7 + lw * 4, 1);
            const u32 wds = (u32)(lw * NSTEP * 16) * 1024u + (u32)c2 * 128;
            auto wd_run = [&](int j0, int n) {
#pragma unroll
                for (int j = 0; j < n; ++j) {
                    const u32 so = wds + (u32)KBLK(j0 + j) * 16384u;
                    dma16(rwd, slot_addr(j0) + lw * 2048 + j * 4096, wdv, so);
                    dma16(rwd, slot_addr(j0) + lw * 2048 + j * 4096 + 1024, wdv, so + 8192u);
                }
            };
            LDS_WAIT(ctl_min2(C_ISS) >= 1);
            wd_run(12, 3);
            WAIT_VM(18);
            ctl_set(C_LANDED + lw * 4, 7);
            NEED_DONE(6);
            wd_run(0, 4);
            WAIT_VM(14);
            ctl_set(C_LANDED + lw * 4, 8);
            TS(2);
            NEED_DONE(7);
            wd_run(4, 4);
            TS(9);
            NEED_DONE(8);
            wd_run(8, 4); wd_run(15, 6);
            TS(10);
            NEED_DONE(10);
            wd_run(21, 6);
            TS(11);
            WAIT_VM(0);
            ctl_set(C_WDA + lw * 4, 1);
            TS(3);
            LDS_WAIT(ctl_get(C_CONVOK + ch * 4) >= 1);
            __builtin_amdgcn_s_setprio(0);
        } else {
            __builtin_amdgcn_s_setprio(3);
            const u64 ep = __hip_atomic_load((const GAS u64*)(a.ws + WS_EPOCH), RLX_AGENT);
            u32 tch0 = 0, tch1 = 0;
            const rsrc_t rk = mk_rsrc(a.k, S * D * 2), rv = mk_rsrc(a.v, S * D * 2);
            const int rblk = xcd * 8 + (l >> 3) * 2 + ch, R0 = rblk * 32, kgrp = l & 7, cg = lane & 7, rq4 = lane >> 3;
            u32x4 kk[7], vv[4], sw4[4];
#pragma unroll
            for (int i = 0; i < 4; ++i) sw4[i] = *(const GAS u32x4*)(a.sk + (kgrp * 64 + cg * 8) * 4 + i * 8);
#pragma unroll
            for (int i = 0; i < 7; ++i) {
                int row = R0 + 4 * rq4 + i - 3;
                u32 off = row >= 0 ? (u32)(row * 1024 + kgrp * 128 + cg * 16) : OOB;
                kk[i] = __builtin_amdgcn_raw_buffer_load_b128(rk, off, 0, 0);
            }
#pragma unroll
            for (int i = 0; i < 4; ++i) vv[i] = __builtin_amdgcn_raw_buffer_load_b128(rv, (u32)((R0 + 4 * rq4 + i) * 1024 + kgrp * 128 + cg * 16), 0, 0);
            __builtin_amdgcn_s_setprio(0);
            if (ch == 0) {
                const u64 t = (((u64)rfl((u32)(ep >> 32)) << 32) | rfl((u32)ep)) + 0x9E3779B97F4A7C15ull;
                *(volatile LAS u32x2*)lp(L_CTL + C_TAG) = (u32x2){(u32)t, (u32)(t >> 32)};
                ctl_set(C_TAGOK, 1);
            }
            float tw[8][4];
#pragma unroll
            for (int c = 0; c < 4; ++c) {
                tw[2 * c][0] = bflo(sw4[c][0]); tw[2 * c][1] = bfhi(sw4[c][0]); tw[2 * c][2] = bflo(sw4[c][1]); tw[2 * c][3] = bfhi(sw4[c][1]);
                tw[2 * c + 1][0] = bflo(sw4[c][2]); tw[2 * c + 1][1] = bfhi(sw4[c][2]); tw[2 * c + 1][2] = bflo(sw4[c][3]); tw[2 * c + 1][3] = bfhi(sw4[c][3]);
            }
#pragma unroll
            for (int c = 0; c < 8; ++c) asm volatile("" : "+v"(tw[c][0]), "+v"(tw[c][1]), "+v"(tw[c][2]), "+v"(tw[c][3]));
            WAIT_VM(0);
            TS(10);
            const u32 scr = L_REST + (u32)ch * 4096;
#pragma unroll
            for (int i = 0; i < 4; ++i) {
                u32x4 o;
#pragma unroll
                for (int c = 0; c < 4; ++c) {
                    float e0 = smul(bflo(kk[i][c]), tw[2 * c][3]);
                    float e1 = smul(bfhi(kk[i][c]), tw[2 * c + 1][3]);
                    e0 = sfma(bflo(kk[i + 1][c]), tw[2 * c][2], e0);
                    e1 = sfma(bfhi(kk[i + 1][c]), tw[2 * c + 1][2], e1);
                    e0 = sfma(bflo(kk[i + 2][c]), tw[2 * c][1], e0);
                    e1 = sfma(bfhi(kk[i + 2][c]), tw[2 * c + 1][1], e1);
                    e0 = sfma(bflo(kk[i + 3][c]), tw[2 * c][0], e0);
                    e1 = sfma(bfhi(kk[i + 3][c]), tw[2 * c + 1][0], e1);
                    o[c] = pk2(smul(e0, bflo(vv[i][c])), smul(e1, bfhi(vv[i][c])));
                }
                lds_w128(scr + (u32)(4 * rq4 + i) * 128 + cg * 16, o);
            }
            CFENCE();
            {
                const int gq = lane >> 4, q4 = (lane & 15) >> 2, p4 = lane & 3;
#pragma unroll
                for (int i = 0; i < 4; ++i) {
                    u32x2 t0 = lds_tr(scr + (u32)(8 * gq + 7 - q4) * 128 + i * 32 + p4 * 8);
                    u32x2 t1 = lds_tr(scr + (u32)(8 * gq + 3 - q4) * 128 + i * 32 + p4 * 8);
                    u32x4 o = {t0[0], t0[1], t1[0], t1[1]};
                    __builtin_amdgcn_raw_buffer_store_b128(o, rws, (u32)((lane & 15) * 64 + gq * 16) + WS_KV + (u32)(rblk * 512 + kgrp * 64 + i * 16) * 64u, 0, SC1);
                }
            }
            SB0();
            TS(11);
            LDS_WAIT(ctl_get(C_TAGOK) >= 1);
            const u64 tag = lds_tag();
            WAIT_VM(0);
            set_flag(a.ws, WS_F0 + (u32)(kgrp * 64 + rblk) * FL, tag, lane);
            ctl_set(C_ISS + ch * 4, 1);
            const u32 wdt = __builtin_amdgcn_raw_buffer_load_b32(rwd, lane < 22 ? (u32)((bid * 2 + ch) * 22 + lane) * 128u : OOB, 0, 0);
            TS(9);
            LDS_WAIT(ctl_min2(C_W7) >= 1);
            const u32x4 gwv = *(const GAS u32x4*)(a.gw + lane * 8);
            u32x4 qg[4];
            const int grow = rt * 128 + ct * 8 + ch * 4;
#pragma unroll
            for (int i = 0; i < 4; ++i) qg[i] = __builtin_amdgcn_raw_buffer_load_b128(rq, (u32)lane * 16u, (u32)(grow + i) * 1024u, 0);
            const rsrc_t rh = mk_rsrc(a.ssmk + (size_t)dch * S, S * 2);
            u32x4 hc[4]; u32x2 hn[4];
#pragma unroll
            for (int i = 0; i < 4; ++i) {
                hc[i] = __builtin_amdgcn_raw_buffer_load_b128(rh, (u32)(lane * 16 + i * 1024), 0, 0);
                hn[i] = __builtin_amdgcn_raw_buffer_load_b64(rh, (u32)(lane * 16 + i * 1024 + 16), 0, 0);
            }
            SB0();
            TS(1);
            poll_flags(a.ws, WS_F0 + (u32)(xcd * 64 + lane) * FL, tag);
            u32x4 uu[4];
#pragma unroll
            for (int i = 0; i < 4; ++i) uu[i] = __builtin_amdgcn_raw_buffer_load_b128(rws, (u32)((lane >> 2) + 16 * i) * 32768u + (u32)(lane & 3) * 16u, WS_KV + (u32)dch * 64u, SC1);
            TS(2);
            {
                float z[4];
#pragma unroll
                for (int i = 0; i < 4; ++i) {
                    float s = smul(bflo(qg[i][0]), bflo(gwv[0]));
                    s = sfma(bfhi(qg[i][0]), bfhi(gwv[0]), s);
#pragma unroll
                    for (int c = 1; c < 4; ++c) { s = sfma(bflo(qg[i][c]), bflo(gwv[c]), s); s = sfma(bfhi(qg[i][c]), bfhi(gwv[c]), s); }
                    s = dpp_add<0x111>(s); s = dpp_add<0x112>(s); s = dpp_add<0x114>(s); s = dpp_add<0x118>(s);
                    s = dpp_add<0x142, 0xa>(s); s = dpp_add<0x143, 0xc>(s);
                    z[i] = __builtin_bit_cast(float, __builtin_amdgcn_readlane(__builtin_bit_cast(u32, s), 63));
                }
                const float z01 = (lane & 1) ? z[1] : z[0], z23 = (lane & 1) ? z[3] : z[2];
                const float zz = rbf((lane & 2) ? z23 : z01);
                const float g = rbf(__builtin_amdgcn_rcpf(1.f + __builtin_amdgcn_exp2f(-1.4426950408889634f * zz)));
                if (lane < 4) __builtin_amdgcn_raw_buffer_store_b32(__builtin_bit_cast(u32, g), rws, (u32)(grow + lane) * 4u, WS_G, SC1);
            }
            asm volatile("" ::"v"(wdt), "v"(cpf), "v"(tch0), "v"(tch1));
            u32x4 t1[4], t2[4], t3[4];
#pragma unroll
            for (int i = 0; i < 4; ++i) {
                t1[i] = (u32x4){__builtin_amdgcn_alignbit(hc[i][1], hc[i][0], 16), __builtin_amdgcn_alignbit(hc[i][2], hc[i][1], 16), __builtin_amdgcn_alignbit(hc[i][3], hc[i][2], 16), __builtin_amdgcn_alignbit(hn[i][0], hc[i][3], 16)};
                t2[i] = (u32x4){smov(hc[i][1]), smov(hc[i][2]), smov(hc[i][3]), smov(hn[i][0])};
                t3[i] = (u32x4){smov(t1[i][1]), smov(t1[i][2]), smov(t1[i][3]), __builtin_amdgcn_alignbit(hn[i][1], hn[i][0], 16)};
                asm volatile("" : "+v"(t1[i]), "+v"(t2[i]), "+v"(t3[i]));
            }
            LDS_WAIT(ctl_min4(C_DONE) >= (u32)(6 + ch));
#pragma unroll
            for (int i = 0; i < 4; ++i) {
                const u32 o = cb + 32 + (u32)(lane * 16 + i * 1024);
                lds_w128(o, hc[i]);
                lds_w128(o + TOEP_CP, t1[i]);
                lds_w128(o + 2 * TOEP_CP, t2[i]);
                lds_w128(o + 3 * TOEP_CP, t3[i]);
            }
            {
                const int sg = lane >> 1, half = lane & 1;
                const u32 d0 = rfl(hc[0][0]), a01 = rfl(t1[0][0]);
                const u32 w3 = !half ? 0u : sg == 1 ? d0 << 16 : sg == 2 ? d0 : sg == 3 ? a01 : 0u;
                const u32 w2 = (half && sg == 3) ? d0 << 16 : 0u;
                if (lane < 8) lds_w128(cb + sg * TOEP_CP + half * 16, (u32x4){0u, 0u, w2, w3});
                if (lane < 30) lds_w128(cb + U_OFF + lane * 16, (u32x4){0u, 0u, 0u, 0u});
            }
#pragma unroll
            for (int i = 0; i < 4; ++i) lds_w128(cb + U_OFF + 480 + lane * 16 + i * 1024, uu[i]);
            ctl_set(C_CONVOK + ch * 4, 1);
            WAIT_VM(0);
            ctl_set(C_HP + ch * 4, 1);
            TS(3);
        }
        const u64 tag = lds_tag();
        const float Dv = bflo(a.ssmD[dch]);
        const int esw = lw & 1, edt = lw >> 1, r32 = lane & 31, hh = lane >> 5;
        const u32 qo = (u32)((row2 + 32 * esw + r32) * 1024 + c2 * 128 + edt * 64 + hh * 32);
        const u32x4 q0 = __builtin_amdgcn_raw_buffer_load_b128(rq, qo, 0, 0), q1 = __builtin_amdgcn_raw_buffer_load_b128(rq, qo + 16, 0, 0);
        {
            const int hf = lw >> 1;
            const int r = lane & 15, kg = lane >> 4;
            const int w0 = 9 + r - 8 * kg;
            const int sg = (w0 + 16) & 3;
            u32 aaddr = cb + (u32)sg * TOEP_CP + (u32)(w0 + 16 - sg) * 2 + hf * 256;
            u32 baddr = cb + U_OFF + 480 + (u32)(8 * kg + 16 * r - 16) * 2 - hf * 256 - 192;
            asm volatile("" : "+v"(aaddr), "+v"(baddr));
            f32x4 acc[8];
#pragma unroll
            for (int i = 0; i < 8; ++i) acc[i] = (f32x4){0.f, 0.f, 0.f, 0.f};
            bf16x8 A[2][8], B[2][8];
            auto ld = [&](int rho) {
#pragma unroll
                for (int pi = 0; pi < 8; ++pi) {
                    constexpr int LO[8] = {3, 4, 2, 1, 5, 0, 6, 7};
                    const int p = LO[pi];
                    u32x2 lo = *(volatile LAS u32x2*)lp(aaddr + rho * 64 + p * 512), hi = *(volatile LAS u32x2*)lp(aaddr + rho * 64 + p * 512 + 8);
                    A[rho & 1][p] = cat(lo, hi);
                    B[rho & 1][p] = asb(lds_r128(baddr + (3 - rho) * 64 + p * 512));
                }
            };
            auto mm = [&](int rho) {
                constexpr int OP[36] = {3, 3, 2, 2, 4, 2, 3, 1, 2, 4, 1, 1, 5, 1, 4, 3, 1, 5, 2, 1, 6, 0, 0, 0, 0, 0, 0, 0, 7, 6, 5, 4, 3, 2, 1, 0};
                constexpr int OQ[36] = {4, 3, 3, 2, 3, 4, 2, 3, 5, 2, 4, 2, 2, 5, 1, 1, 6, 1, 1, 1, 1, 6, 5, 4, 7, 3, 2, 1, 0, 0, 0, 0, 0, 0, 0, 0};
#pragma unroll
                for (int n = 0; n < 36; ++n) acc[OP[n] + OQ[n]] = MFMA16(A[rho & 1][OP[n]], B[rho & 1][OQ[n]], acc[OP[n] + OQ[n]]);
            };
            auto pat = [&]() {
#pragma unroll
                for (int i = 0; i < 12; ++i) { SGB(0x8, 1); SGB(0x100, 1); SGB(0x8, 1); SGB(0x100, 1); SGB(0x8, 1); }
            };
            ld(0);
            LDS_WAIT(ctl_get(C_GEG + lw * 4) >= 1);
            __builtin_amdgcn_s_setprio(CVPRIO);
            TS(4);
            WAIT_LGKM0();
            SB0();
            ld(1); mm(0); pat(); SB0();
            ld(2); mm(1); pat(); SB0();
            ld(3); mm(2); pat(); SB0();
            ctl_set(C_CDONE + lw * 4, 1);
            u32x2 uv[4];
            {
                const u32 uaddr = cb + U_OFF + 480 + (u32)(16 * r + 8 * (kg >> 1) + 4 * (1 - (kg & 1))) * 2 + hf * 2048;
#pragma unroll
                for (int i = 0; i < 4; ++i) uv[i] = *(volatile LAS u32x2*)lp(uaddr + i * 512);
            }
            SB0();
            mm(3); SB0();
            LDS_SPIN(ctl_get(C_CDONE + (lw ^ 2) * 4) >= 1);
#pragma unroll
            for (int i = 0; i < 4; ++i) lds_w128(cb + (u32)hf * 4096 + i * 1024 + lane * 16, __builtin_bit_cast(u32x4, hf ? acc[i] : acc[4 + i]));
            ctl_set(C_CDONE + lw * 4, 2);
            float kd[4][4];
#pragma unroll
            for (int i = 0; i < 4; ++i) { kd[i][0] = rbf(smul(bfhi(uv[i][1]), Dv)); kd[i][1] = rbf(smul(bflo(uv[i][1]), Dv)); kd[i][2] = rbf(smul(bfhi(uv[i][0]), Dv)); kd[i][3] = rbf(smul(bflo(uv[i][0]), Dv)); }
            LDS_SPIN(ctl_get(C_CDONE + (lw ^ 2) * 4) >= 2);
            f32x4 pp[4];
#pragma unroll
            for (int i = 0; i < 4; ++i) pp[i] = __builtin_bit_cast(f32x4, lds_r128(cb + (u32)(hf ^ 1) * 4096 + i * 1024 + lane * 16));
            u32x2 yo[4];
#pragma unroll
            for (int i = 0; i < 4; ++i) {
                const f32x4 m = hf ? acc[4 + i] : acc[i];
                const u32 r01 = pk2(sadd(m[0], pp[i][0]), sadd(m[1], pp[i][1])), r23 = pk2(sadd(m[2], pp[i][2]), sadd(m[3], pp[i][3]));
                yo[i] = (u32x2){pk2(sadd(bflo(r01), kd[i][0]), sadd(bfhi(r01), kd[i][1])), pk2(sadd(bflo(r23), kd[i][2]), sadd(bfhi(r23), kd[i][3]))};
            }
#pragma unroll
            for (int i = 0; i < 4; i += 2) {
                const auto s0 = __builtin_amdgcn_permlane16_swap(yo[i][0], yo[i + 1][0], false, false);
                const auto s1 = __builtin_amdgcn_permlane16_swap(yo[i][1], yo[i + 1][1], false, false);
                const u32x4 o = {s0[0], s1[0], s0[1], s1[1]};
                __builtin_amdgcn_raw_buffer_store_b128(o, rws, (u32)(32 * r + 16 * (kg >> 1) + (i + (kg & 1)) * 512) + WS_Y + (u32)dch * 4096u + (u32)hf * 2048u, 0, SC1);
            }
            TS(5);
            WAIT_VM(0);
            set_flag(a.ws, WS_F2 + (u32)(((xcd * 2 + hf) * 32 + l) * 2 + ch) * FL, tag, lane);
            TS(6);
        }
        LDS_WAIT(ctl_get(C_HRDY) >= 1);
        const float gval = __builtin_bit_cast(float, __builtin_amdgcn_raw_buffer_load_b32(rws, (u32)(row2 + 32 * esw + r32) * 4u, WS_G, SC1));
        TS(7);
        {
            const GAS u64* p = (const GAS u64*)(a.ws + WS_F2 + (u32)((c2 * 2 + (row2 >> 10)) * 64 + lw * 16 + (lane & 15)) * FL);
            bool ok = lane >= 16;
#pragma unroll 1
            for (u32 sp = 0; sp < SPIN_CAP; ++sp) {
                if (!ok) ok = __hip_atomic_load(p, RLX_AGENT) == tag;
                if (__all(ok)) break;
                __builtin_amdgcn_s_sleep(PSLEEP);
            }
            ctl_set(C_WDB + lw * 4, 1);
            LDS_SPIN(ctl_min2(C_WDB + edt * 8) >= 1);
        }
        TS(8);
        const u32 yo = WS_Y + (u32)(c2 * 64 + edt * 32) * 4096u + (u32)(row2 + 32 * esw) * 2u;
        const u32x4 y0 = __builtin_amdgcn_raw_buffer_load_b128(rws, (u32)(lane >> 2) * 4096u + (u32)(lane & 3) * 16u, yo, SC1);
        const u32x4 y1 = __builtin_amdgcn_raw_buffer_load_b128(rws, (u32)((lane >> 2) + 16) * 4096u + (u32)(lane & 3) * 16u, yo, SC1);
        {
            const u32 ys = cb + YST_OFF + (u32)edt * 2048;
            lds_w128(ys + lane * 16, y0);
            lds_w128(ys + 1024 + lane * 16, y1);
            CFENCE();
            const int q4 = (lane & 15) >> 2, p4 = lane & 3, g1 = (lane >> 4) & 1;
            u32 go[8];
#pragma unroll
            for (int i = 0; i < 4; ++i) {
                u32x2 yv = lds_tr(ys + (u32)(16 * hh + 4 * i + q4) * 64 + g1 * 32 + p4 * 8);
                const u32 qa = i < 2 ? q0[2 * i] : q1[2 * i - 4], qb = i < 2 ? q0[2 * i + 1] : q1[2 * i - 3];
                const u32 pa = pk2(smul(bflo(qa), bflo(yv[0])), smul(bfhi(qa), bfhi(yv[0]))), pb = pk2(smul(bflo(qb), bflo(yv[1])), smul(bfhi(qb), bfhi(yv[1])));
                go[2 * i] = pk2(smul(gval, bflo(pa)), smul(gval, bfhi(pa)));
                go[2 * i + 1] = pk2(smul(gval, bflo(pb)), smul(gval, bfhi(pb)));
            }
            lds_w128(cb + GA_OFF + (u32)edt * 2048 + lane * 16, (u32x4){go[0], go[1], go[2], go[3]});
            lds_w128(cb + GA_OFF + (u32)edt * 2048 + 1024 + lane * 16, (u32x4){go[4], go[5], go[6], go[7]});
        }
        if (bid == 0 && lw == 3) {
            const GAS u64* p = (const GAS u64*)(a.ws + WS_F0 + (u32)lane * 2 * FL);
#pragma unroll 1
            for (u32 sp = 0; sp < SPIN_CAP; ++sp) {
                u64 x0 = __hip_atomic_load(p, RLX_AGENT), x1 = __hip_atomic_load(p + 16 * FL, RLX_AGENT), x2 = __hip_atomic_load(p + 32 * FL, RLX_AGENT), x3 = __hip_atomic_load(p + 48 * FL, RLX_AGENT);
                if (sp == 0) BARRIER();
                if (__all(x0 == tag && x1 == tag && x2 == tag && x3 == tag)) break;
                __builtin_amdgcn_s_sleep(8);
            }
            set_flag(a.ws, WS_EPOCH, tag, lane);
        } else {
            BARRIER();
        }
    }
}

extern "C" void kernel_launch(void* const* d_in, const int* in_sizes, int n_in,
                              void* d_out, int out_size, void* d_ws, size_t ws_size,
                              hipStream_t stream) {
    Args a;
    a.q = (const u16*)d_in[0]; a.k = (const u16*)d_in[1]; a.v = (const u16*)d_in[2];
    a.sk = (const u16*)d_in[3]; a.ssmk = (const u16*)d_in[4]; a.ssmD = (const u16*)d_in[5];
    a.wg = (const u16*)d_in[7]; a.wu = (const u16*)d_in[8]; a.wd = (const u16*)d_in[9];
    a.gw = (const u16*)d_in[10];
    a.out = (u16*)d_out; a.ws = (char*)d_ws;
    fused_kernel<<<256, 512, 0, stream>>>(a);
}
```

```cpp
#include <hip/hip_runtime.h>
#include <stdint.h>

typedef unsigned short u16;
typedef unsigned int u32;
typedef unsigned long long u64;
typedef short s16x4 __attribute__((ext_vector_type(4)));
typedef __bf16 bf16x8 __attribute__((ext_vector_type(8)));
typedef __bf16 bf16x2 __attribute__((ext_vector_type(2)));
typedef float f32x2 __attribute__((ext_vector_type(2)));
typedef float f32x4 __attribute__((ext_vector_type(4)));
typedef float f32x16 __attribute__((ext_vector_type(16)));
typedef u32 u32x2 __attribute__((ext_vector_type(2)));
typedef u32 u32x4 __attribute__((ext_vector_type(4)));
#define LAS __attribute__((address_space(3)))
#define GAS __attribute__((address_space(1)))
#define DI __device__ __forceinline__
#define RLX_AGENT __ATOMIC_RELAXED, __HIP_MEMORY_SCOPE_AGENT
#define SC1 16

constexpr int S = 2048, D = 512, F = 1376;

constexpr u32 WS_EPOCH = 0;
constexpr u32 WS_G = 4096;
constexpr u32 FL = 128;
constexpr u32 WS_F0 = 65536;
constexpr u32 WS_F1 = 131072;
constexpr u32 WS_F2 = 262144;
constexpr u32 WS_F3 = 393216;
constexpr u32 WS_KV = 1u << 20;
constexpr u32 WS_Y = 3u << 20;
constexpr u32 WS_H = 5u << 20;
constexpr u32 WS_END = WS_H + 64 * 86 * 1024;
constexpr u32 WS_DBG = 16u << 20;

constexpr u32 L_CTL = 0;
constexpr u32 L_X = 4096;
constexpr u32 L_W = 53248;
constexpr u32 WBUF = 24576, WU_OFF = 12288;
constexpr u32 L_REST = L_W + 4 * WBUF;
constexpr u32 L_CONV = L_W + 2 * WBUF;
constexpr u32 TOEP_CP = 4160;
constexpr u32 U_OFF = 4 * TOEP_CP;
constexpr u32 LDS_BYTES = 163840;
constexpr u32 C_LANDED = 0;
constexpr u32 C_ISS = 8;
constexpr u32 C_DONE = 16;
constexpr u32 C_HP = 32;
constexpr u32 C_WDA = 40;
constexpr u32 C_CDONE = 48;
constexpr u32 C_WDB = 64;
constexpr u32 C_G2 = 80;
constexpr u32 C_S0 = 96;
constexpr u32 C_GEG = 112;
constexpr u32 C_TAG = 128;
constexpr u32 C_TAGOK = 136;
constexpr u32 C_HRDY = 140;
constexpr u32 C_LND = 192;
constexpr u32 C_CNT = 512;
constexpr u32 C_OMG = 256;
constexpr u32 C_W7 = 152;
constexpr u32 C_CONVOK = 160;

__device__ __forceinline__ constexpr u32 slot_addr(int j) {
    if (j >= 27) j -= 27;
    if (j < 4) return L_X + 2 * 16384 + j * 4096;
    if (j < 8) return L_X + (j - 4) * 4096;
    if (j < 12) return L_X + 16384 + (j - 8) * 4096;
    if (j < 15) return L_REST + (j - 12) * 4096;
    if (j < 21) return L_W + (j - 15) * 4096;
    return L_W + WBUF + (j - 21) * 4096;
}
constexpr int NA = 27, NSTEP = 43;
constexpr u32 YST_OFF = 8192;
constexpr u32 GA_OFF = 12288;
constexpr u32 L_EXCH = L_W + 4096;

#define ROT 0
#define PSLEEP 2
#define LATER 6
#define TOUCH 1
#define WL0 5
#define WLN 5
#define LDPRIO 0
#define R2MUL 0
#define G2PRIO 1
#define CVPRIO 2
#define R2X 0
#define LVPOS 17
#define KVAT 0
#define WLE 8
#define ROTS 0
#define F2AT 2
#define HPW 3
#define WLS 4
#define WLG 2
#define HPAT 5
#define TWAIT 0
#define GATEAT 5
#define PDELAY 0
#define LDW1 0
#define KVD 10
#define HWIN 16
constexpr u32 CODE_BYTES = 27136;
constexpr u32 OOB = 0x7ffffff0u;
constexpr u32 SPIN_CAP = 1u << 16;

struct Args {
    const u16 *q, *k, *v, *sk, *ssmk, *ssmD, *wg, *wu, *wd, *gw;
    u16* out;
    char* ws;
};

#define TS(i) do { } while (0)

DI float bflo(u32 x) { return __uint_as_float(x << 16); }
DI float bfhi(u32 x) { return __uint_as_float(x & 0xffff0000u); }
DI u32 pk2(float a, float b) { f32x2 p = {a, b}; return __builtin_bit_cast(u32, __builtin_convertvector(p, bf16x2)); }
DI float sfma(float a, float b, float c) { float r; asm("v_fma_f32 %0, %1, %2, %3" : "=v"(r) : "v"(a), "v"(b), "v"(c)); return r; }
DI float smul(float a, float b) { float r; asm("v_mul_f32 %0, %1, %2" : "=v"(r) : "v"(a), "v"(b)); return r; }
DI float sadd(float a, float b) { float r; asm("v_add_f32 %0, %1, %2" : "=v"(r) : "v"(a), "v"(b)); return r; }
DI u32 smov(u32 a) { u32 r; asm("v_mov_b32 %0, %1" : "=v"(r) : "v"(a)); return r; }
DI float rbf(float a) { return bflo(pk2(a, a)); }
DI u32 rfl(u32 x) { return __builtin_amdgcn_readfirstlane(x); }
DI LAS char* lp(u32 off) { return (LAS char*)(uintptr_t)off; }
DI u32x4 lds_r128(u32 a) { return *(LAS u32x4*)lp(a); }
DI u32x2 lds_r64(u32 a) { return *(LAS u32x2*)lp(a); }
DI void lds_w128(u32 a, u32x4 v) { *(LAS u32x4*)lp(a) = v; }
DI u32x2 lds_tr(u32 a) { return __builtin_bit_cast(u32x2, __builtin_amdgcn_ds_read_tr16_b64_v4i16((LAS s16x4*)lp(a))); }
DI bf16x8 cat(u32x2 a, u32x2 b) { u32x4 v = {a[0], a[1], b[0], b[1]}; return __builtin_bit_cast(bf16x8, v); }
DI bf16x8 asb(u32x4 v) { return __builtin_bit_cast(bf16x8, v); }
#define CFENCE() asm volatile("" ::: "memory")
#define WAIT_VM(n) asm volatile("s_waitcnt vmcnt(%0)" ::"i"(n) : "memory")
#define WAIT_LGKM0() asm volatile("s_waitcnt lgkmcnt(0)" ::: "memory")
#define BARRIER() asm volatile("s_waitcnt lgkmcnt(0)\n\ts_barrier" ::: "memory")
DI void ctl_set(u32 off, u32 v) { CFENCE(); *(volatile LAS u32*)lp(L_CTL + off) = v; CFENCE(); }
DI u32 ctl_min2(u32 off) { u32x2 v = *(volatile LAS u32x2*)lp(L_CTL + off); return rfl(min(v[0], v[1])); }
DI u32 ctl_min4(u32 off) { u32x4 v = *(volatile LAS u32x4*)lp(L_CTL + off); return rfl(min(min(v[0], v[1]), min(v[2], v[3]))); }
DI u32 ctl_get(u32 off) { return rfl(*(volatile LAS u32*)lp(L_CTL + off)); }
#define LDS_WAIT(cond) do { u32 _sp = 0; _Pragma("unroll 1") while (!(cond)) { __builtin_amdgcn_s_sleep(1); if (++_sp > SPIN_CAP * 4) break; } CFENCE(); } while (0)
#define LDS_SPIN(cond) do { u32 _sp = 0; _Pragma("unroll 1") while (!(cond)) { if (++_sp > SPIN_CAP * 4) break; } CFENCE(); } while (0)

#define ARRIVE(n) asm volatile("s_mov_b64 exec, %1\n\tds_append %0 offset:%3\n\ts_mov_b64 exec, %2\n\tds_append %0 offset:%4\n\ts_mov_b64 exec, -1" \
                               : "+v"(apd) : "s"(em0), "s"(em1), "i"(L_CTL + C_CNT), "i"(L_CTL + C_CNT + 4) : "memory")
DI u32 arrived_min() {
    const u32x2 v = *(volatile LAS u32x2*)lp(L_CTL + C_CNT);
    const u32 a = rfl(v[0]), b = rfl(v[1]);
    return min(min(a & 15u, a >> 4), min(b & 15u, b >> 4));
}
#define ARRIVED(n) (arrived_min() >= (u32)(n))
DI void poll_flags(const char* ws, u32 byte_off, u64 tag) {
    const GAS u64* p = (const GAS u64*)(ws + byte_off);
    bool ok = false;
#pragma unroll 1
    for (u32 sp = 0; sp < SPIN_CAP; ++sp) {
        if (!ok) ok = __hip_atomic_load(p, RLX_AGENT) == tag;
        if (__all(ok)) break;
        __builtin_amdgcn_s_sleep(PSLEEP);
    }
    CFENCE();
}
DI void set_flag(char* ws, u32 byte_off, u64 tag, int lane) {
    if (lane == 0) __hip_atomic_store((GAS u64*)(ws + byte_off), tag, RLX_AGENT);
}
DI u64 lds_tag() { u32x2 v = *(volatile LAS u32x2*)lp(L_CTL + C_TAG); return ((u64)rfl(v[1]) << 32) | rfl(v[0]); }
typedef __amdgpu_buffer_rsrc_t rsrc_t;
DI rsrc_t mk_rsrc(const void* p, u32 bytes) { return __builtin_amdgcn_make_buffer_rsrc((void*)p, 0, bytes, 0x00020000); }
DI void dma16(rsrc_t r, u32 lds, u32 voff, u32 soff) { __builtin_amdgcn_raw_ptr_buffer_load_lds(r, (LAS void*)lp(lds), 16, voff, soff, 0, 0); }
#define DMA16O(r, lds, voff, soff, imm) __builtin_amdgcn_raw_ptr_buffer_load_lds(r, (LAS void*)lp(lds), 16, voff, soff, imm, 0)
#define SGB(mask, n) __builtin_amdgcn_sched_group_barrier(mask, n, 0)
#define SB0() __builtin_amdgcn_sched_barrier(0)
#define MFMA32(a, b, c) __builtin_amdgcn_mfma_f32_32x32x16_bf16(a, b, c, 0, 0, 0)
#define MFMA16(a, b, c) __builtin_amdgcn_mfma_f32_16x16x32_bf16(a, b, c, 0, 0, 0)
template <int CTRL, int RM = 0xf> DI float dpp_add(float x) { return x + __builtin_bit_cast(float, __builtin_amdgcn_update_dpp(0, __builtin_bit_cast(int, x), CTRL, RM, 0xf, true)); }

__global__ __launch_bounds__(512, 2) void fused_kernel(Args a) {
    __shared__ __attribute__((aligned(128))) char smem[LDS_BYTES];
    const int tid = threadIdx.x, lane = tid & 63;
    const int wave = rfl(tid >> 6);
    const int bid = blockIdx.x, xcd = bid & 7, l = bid >> 3;
    TS(0);
    u32 cpf = 0;
    if (wave == 7) {
        u64 pc;
        asm volatile("s_getpc_b64 %0" : "=s"(pc));
        const rsrc_t rc = mk_rsrc((const void*)(pc & ~255ull), CODE_BYTES);
        cpf = __builtin_amdgcn_raw_buffer_load_b32(rc, (u32)(((l & 7) * 64 + lane) * 128), 0, 0);
    }
    if (wave == 6) { ((volatile LAS u32*)((LAS char*)smem + L_CTL))[lane] = lane >= 48; ((volatile LAS u32*)((LAS char*)smem + L_CTL + C_CNT))[lane] = 0; ((volatile LAS char*)smem)[LDS_BYTES - 64 + lane] = 0; }

    const int rt = (xcd & 1) * 8 + (l & 7), cq = xcd >> 1, cj = l >> 3, ct = cq * 4 + cj;
    const int unit0 = 11 * cq + 3 * cj;
    const int nunits = min(3, min(11 * cq + 11, 43) - unit0);
    const int rot = ((l + cj) & ROT) << ROTS;
    const int row2 = (xcd & 1) * 1024 + (xcd >> 1) * 256 + (l & 3) * 64, c2 = (l >> 2) == 3 ? 6 : (l >> 2) == 6 ? 3 : l >> 2;

    const int r2 = ((l & 3) * R2MUL + xcd * R2X) % NSTEP;
#define KBLK(j) ((j) + r2 >= NSTEP ? (j) + r2 - NSTEP : (j) + r2)

    const rsrc_t rws = mk_rsrc(a.ws, WS_END);
    const rsrc_t rq = mk_rsrc(a.q, S * D * 2);
    const rsrc_t rwd = mk_rsrc(a.wd, F * D * 2);

    const rsrc_t rw = mk_rsrc((wave & 1) ? a.wu : a.wg, F * D * 2);
    u32 tch0 = 0, tch1 = 0;
    if (TOUCH && wave >= 6) {
        const int ux = ((cq * 32 + l) * 2 + (wave & 1)) * 32 + lane;
        tch0 = __builtin_amdgcn_raw_buffer_load_b32(rq, lane < 32 ? (u32)(((xcd & 1) * 1024 + (ux & 1023)) * 1024 + (ux >> 10) * 128) : OOB, 0, 0);
        const int r = (xcd & 1) * 32 + l + 64 * (lane >> 3), c = lane & 7;
        tch1 = __builtin_amdgcn_raw_buffer_load_b32(rw, c < 7 ? (u32)(r * (F * 2) + cq * 704 + c * 128 - (c == 6 ? 68 : 0)) : OOB, 0, 0);
    }
    const u32 xv0 = (u32)(lane >> 3) * 1024 + (u32)(((lane & 7) ^ (lane >> 4)) * 16);
    const u32 xv1 = xv0 ^ 64;
    u32 wv[3];
#pragma unroll
    for (int m = 0; m < 3; ++m) {
        int n = 64 * m + lane, rr = n / 12, c = n - rr * 12;
        wv[m] = c < 4 * nunits ? (u32)(rr * (F * 2) + (c ^ ((rr >> 2) & 2)) * 16) : OOB;
    }
    const u32 lwb = L_W + ((wave & 1) ? WU_OFF : 0), ws0 = (u32)unit0 * 64;

#define WDV ((u32)(lane >> 3) * 1024 + (u32)(((lane & 7) ^ (((lane >> 4) & 1) << 2)) * 16))

    if (wave < 4) {
        __builtin_amdgcn_s_setprio(2);
        {
            const u32 xs = (u32)(rt * 128 + wave * 32) * 1024, wq = (u32)(wave >> 1);
#pragma unroll
            for (int i = 0; i < 4; ++i) dma16(rq, L_X + wave * 4096 + i * 1024, (i & 1) ? xv1 : xv0, xs + i * 8192 + rot * 128);
#pragma unroll
            for (int i = 0; i < 6; ++i) dma16(rw, lwb + wq * 6144 + i * 1024, wv[i % 3], ws0 + (u32)(rot * 64 + wq * 32 + (i / 3) * 16) * (F * 2));
        }
        BARRIER();
        const int sw = wave & 1, fw = wave >> 1;
        const int r32 = lane & 31, hh = lane >> 5;
        const int r16 = lane & 15, kg = lane >> 4;
        f32x4 acc[4][6];
#pragma unroll
        for (int i = 0; i < 4; ++i)
#pragma unroll
            for (int j = 0; j < 6; ++j) acc[i][j] = (f32x4){0.f, 0.f, 0.f, 0.f};
        const u32 wrow = L_W + (u32)(8 * kg + (r16 >> 2)) * 192 + (u32)((lane & 3) * 8);
        u32 wa[2], wn[2];
        wa[0] = wrow + (((u32)(3 * fw) * 32) ^ ((kg & 1) * 32)); wa[1] = wrow + (((u32)(3 * fw + 1) * 32) ^ ((kg & 1) * 32));
        wn[0] = wa[0] + 2 * WBUF; wn[1] = wa[1] + 2 * WBUF;
        const u32 xa0 = L_X + (u32)(64 * sw + r16) * 128 + (u32)((kg ^ ((r16 >> 1) & 7)) * 16);
        u32x4 xb[2][8];
        u32x2 wb[2][12];
        auto ldx1 = [&](int xs, u32 a0, u32 a1, int n) { xb[xs][n] = lds_r128((n >> 2 ? a1 : a0) + (n & 3) * 2048); };
        auto ldw1 = [&](int buf, int sl, int kk, int n) {
            const int j = n >> 1;
            wb[buf][n] = lds_tr((sl == 2 ? wn : wa)[(j % 3) & 1] + (sl & 1) * WBUF + (j / 3) * WU_OFF + kk * 32 * 192 + ((j % 3) >> 1) * 64 + (n & 1) * 4 * 192);
        };
        auto mm1 = [&](int buf, int xs, int kk, int n) {
            const int j = n >> 2, i = n & 3;
            const u32x4 av = {wb[buf][2 * j][0], wb[buf][2 * j][1], wb[buf][2 * j + 1][0], wb[buf][2 * j + 1][1]};
            asm volatile("v_mfma_f32_16x16x32_bf16 %0, %1, %2, %0" : "+v"(acc[i][j]) : "v"(av), "v"(xb[xs][kk * 4 + i]));
        };
#pragma unroll
        for (int i = 0; i < 4; ++i)
#pragma unroll
            for (int j = 0; j < 6; ++j) asm volatile("" : "+v"(acc[i][j]));
        WAIT_VM(0);
        ctl_set(C_S0 + wave * 4, 1);
        LDS_SPIN(ctl_min4(C_S0) >= 1);
        TS(1);
        WAIT_LGKM0();
#pragma unroll
        for (int n = 0; n < 4; ++n) ldx1(0, xa0, xa0 ^ 64, n);
#pragma unroll
        for (int n = 0; n < 12; ++n) ldw1(0, 0, 0, n);
#pragma unroll
        for (int n = 4; n < 8; ++n) ldx1(0, xa0, xa0 ^ 64, n);
        u32 apd = 0;
        const u64 emk = (wave & 1) ? 0xffffull : 1ull, em0 = (wave & 2) ? 0ull : emk, em1 = (wave & 2) ? emk : 0ull;
        ARRIVE(1);
        SB0();
        const int kh = wave >> 1;
        u32x4 wl[16];
        const u32 wdv = WDV, wds = (u32)(kh * NSTEP * 16 + sw * 8) * 1024u + (u32)c2 * 128;
        auto wload = [&](int n) { if (n >= 0 && n < 16) wl[n] = __builtin_amdgcn_raw_buffer_load_b128(rwd, wdv, wds + (u32)KBLK(NA + n) * 16384u, 0); };
        int xbuf = 1;
        auto stage = [&](int it, int xs) {
            const int s = 2 * it + xs;
            u32 xn0 = xa0 + xbuf * 16384, xn1 = xn0 ^ 64;
            asm volatile("" : "+v"(xn0), "+v"(xn1));
            xbuf = xbuf == 2 ? 0 : xbuf + 1;
            u32x2 lv = {0, 0};
            const u32 lnd = L_CTL + C_LND + (u32)min(s + 1, 7) * 8;
#pragma unroll
            for (int n = 0; n < 24; ++n) {
                mm1(0, xs, 0, n);
                if (n % 3 != 2 && n < 18) ldw1(1, xs, 1, n - n / 3);
                if (n == LVPOS) lv = *(volatile LAS u32x2*)lp(lnd);
                SB0();
            }
            if (rfl(lv[0] | lv[1])) LDS_SPIN(({ u32x2 t = *(volatile LAS u32x2*)lp(lnd); rfl(t[0] | t[1]) == 0; }));
            CFENCE();
#pragma unroll
            for (int n = 0; n < 24; ++n) {
                mm1(1, xs, 1, n);
                if (n < 8) ldx1(xs ^ 1, xn0, xn1, n); else if (n < 20) ldw1(0, xs + 1, 0, n - 8);
                if (n == 7) ARRIVE(s + 2);
                SB0();
            }
        };
#pragma unroll 1
        for (int it = 0; it < 4; ++it) {
            stage(it, 0); stage(it, 1);
            const u32 t0 = wa[0], t1 = wa[1]; wa[0] = wn[0]; wa[1] = wn[1]; wn[0] = t0; wn[1] = t1;
        }
        asm volatile("s_waitcnt lgkmcnt(0)\n\ts_nop 7" : : "v"(apd) : "memory");
        SB0();
        TS(2);
        {
            const float c0 = -2.f * 0.7978845608028654f * 1.4426950408889634f, c1 = c0 * 0.044715f;
            const u32 hv0 = (u32)lane * 16u + WS_H + (u32)((rt * 4 + sw * 2) * 86 + unit0 * 2 + 3 * fw) * 1024u;
#pragma unroll
            for (int j = 0; j < 3; ++j) {
                const u32 hvo = 3 * fw + j < 2 * nunits ? hv0 + j * 1024 : OOB;
#pragma unroll
                for (int i = 0; i < 2; ++i) {
                    u32 hp[4];
#pragma unroll
                    for (int e = 0; e < 4; ++e) {
                        const f32x4 G = acc[2 * i + (e >> 1)][j], U = acc[2 * i + (e >> 1)][3 + j];
                        const f32x2 g = {G[2 * (e & 1)], G[2 * (e & 1) + 1]}, u = {U[2 * (e & 1)], U[2 * (e & 1) + 1]};
                        const f32x2 z = g * __builtin_elementwise_fma(g * g, (f32x2){c1, c1}, (f32x2){c0, c0});
                        const f32x2 dn = (f32x2){__builtin_amdgcn_exp2f(z[0]), __builtin_amdgcn_exp2f(z[1])} + (f32x2){1.f, 1.f};
                        const f32x2 hv = (g * u) * (f32x2){__builtin_amdgcn_rcpf(dn[0]), __builtin_amdgcn_rcpf(dn[1])};
                        hp[e] = pk2(hv[0], hv[1]);
                    }
                    const auto s0 = __builtin_amdgcn_permlane16_swap(hp[0], hp[2], false, false);
                    const auto s1 = __builtin_amdgcn_permlane16_swap(hp[1], hp[3], false, false);
                    const u32x4 o = {s0[0], s1[0], s0[1], s1[1]};
                    __builtin_amdgcn_raw_buffer_store_b128(o, rws, hvo + i * 86 * 1024, 0, SC1);
                    SB0();
#pragma unroll
                    for (int n = 0; n < WLG; ++n) if ((j * 2 + i) * WLG + n < WLE) wload((j * 2 + i) * WLG + n);
                    SB0();
                }
            }
        }
        ctl_set(C_GEG + wave * 4, 1);
        __builtin_amdgcn_s_setprio(0);
        TS(3);
        LDS_WAIT(ctl_get(C_TAGOK) >= 1);
        const u64 tag = lds_tag();
        WAIT_VM(0);
        set_flag(a.ws, WS_F1 + (u32)(((rt * 2 + sw) * 16 + ct) * 2 + fw) * FL, tag, lane);
        TS(4);
        LDS_SPIN(ARRIVED(8));
#pragma unroll
        for (int j = 0; j < (LDW1 ? 4 : 10); ++j) { const int sj = j < 4 ? 8 + j : 17 + j; dma16(rwd, slot_addr(sj) + wave * 1024, wdv, wds + (u32)KBLK(sj) * 16384u); }
#pragma unroll
        for (int n = WLE; n < 16; ++n) wload(n);
        f32x16 c2a[2];
#pragma unroll
        for (int i = 0; i < 2; ++i)
#pragma unroll
            for (int e = 0; e < 16; ++e) c2a[i][e] = 0.f;
        const int q4 = (lane & 15) >> 2, p4 = lane & 3, g1 = (lane >> 4) & 1;
        const int drel = 16 * (p4 & 1) + 4 * (p4 >> 1) + 8 * g1;
        const u32 swz = (u32)((q4 >> 1) & 1) << 6;
        const u32 da0 = (u32)kh * 2048 + (u32)(8 * hh + q4) * 128 + (((u32)drel * 2) ^ swz);
        const u32 da1 = da0 ^ 64;
        bf16x8 a0[4], a1[4];
        constexpr u32 MID = L_W, HI = L_REST;
        u32 da0m = da0 + MID, da1m = da1 + MID, da0h = da0 + HI, da1h = da1 + HI;
        asm volatile("" : "+v"(da0m), "+v"(da1m), "+v"(da0h), "+v"(da1h));
#define SADDR(b, j) (slot_addr(j) < MID ? b + slot_addr(j) : slot_addr(j) < HI ? b##m + (slot_addr(j) - MID) : b##h + (slot_addr(j) - HI))
#define ALOAD(j) do { a0[(j) & 3] = cat(lds_tr(SADDR(da0, j)), lds_tr(SADDR(da0, j) + 512)); \
                      a1[(j) & 3] = cat(lds_tr(SADDR(da1, j)), lds_tr(SADDR(da1, j) + 512)); } while (0)
        if (wave == 0) {
            if (PDELAY) __builtin_amdgcn_s_sleep(PDELAY);
            poll_flags(a.ws, WS_F1 + (u32)((row2 >> 6) * 32 + (lane & 31)) * FL, tag);
            ctl_set(C_HRDY, 1);
        }
        LDS_WAIT(({ u32x2 t = *(volatile LAS u32x2*)lp(L_CTL + C_LND); rfl(t[0] | t[1]) == 0; }));
        WAIT_VM(0);
        ctl_set(C_S0 + wave * 4, 2);
        ALOAD(0); ALOAD(1); ALOAD(2);
        if (wave != 0) LDS_WAIT(ctl_get(C_HRDY) >= 1);
        __builtin_amdgcn_s_setprio(G2PRIO);
        TS(5);
        u32x4 hb[NSTEP];
        const u32 hbase = WS_H + (u32)(((row2 >> 5) + sw) * 86 + kh * NSTEP) * 1024u;
#define HLOAD(j) hb[j] = __builtin_amdgcn_raw_buffer_load_b128(rws, (u32)lane * 16u, hbase + (u32)KBLK(j) * 1024u, SC1)
#pragma unroll
        for (int j = 0; j < HWIN; ++j) { HLOAD(j); SB0(); }
        LDS_SPIN(ctl_get(C_S0 + (wave ^ 1) * 4) >= 2);
        WAIT_LGKM0();
        SB0();
        const u32 wla = (u32)wave * 1024 + (u32)lane * 16;
        u32 wlah = wla + HI;
        const u32 wlam = wla + MID;
        asm volatile("" : "+v"(wlah));
#define G2_RANGE(j0, j1, WOFF) _Pragma("unroll") for (int j = j0; j < j1; ++j) { \
            if (j + HWIN < NSTEP) HLOAD(j + HWIN); \
            if (j + 3 < NSTEP) ALOAD(j + 3); \
            if (WOFF >= 0) lds_w128(SADDR(wla, j - WOFF), wl[j - WOFF]); \
            c2a[0] = MFMA32(a0[j & 3], asb(hb[j]), c2a[0]); \
            c2a[1] = MFMA32(a1[j & 3], asb(hb[j]), c2a[1]); \
            SGB(0x8, 1); SGB(0x100, 2); SGB(0x20, 1); SGB(0x8, 1); SGB(0x100, 2); SGB(0x200, 1); SB0(); }
#define G2_PEEK() const u32 pk = *(volatile LAS u32*)lp(L_CTL + C_G2 + (wave ^ 1) * 4); SB0();
#define G2_NEED(n) if (rfl(pk) < n) LDS_SPIN(ctl_get(C_G2 + (wave ^ 1) * 4) >= n); CFENCE();
        G2_RANGE(0, 5, -1)
        TS(9);
        ctl_set(C_G2 + wave * 4, 1);
        G2_RANGE(5, 6, -1)
        { G2_PEEK() G2_RANGE(6, 8, -1) G2_NEED(1) }
        G2_RANGE(8, 13, 8)
        ctl_set(C_G2 + wave * 4, 2);
        G2_RANGE(13, 14, 8)
        { G2_PEEK() G2_RANGE(14, 16, 8) G2_NEED(2) }
        TS(10);
        ctl_set(C_G2 + wave * 4, 3);
        G2_RANGE(16, 22, 8)
        { G2_PEEK() G2_RANGE(22, 24, 8) G2_NEED(3) }
        ctl_set(C_G2 + wave * 4, 4);
        G2_RANGE(24, 30, -1)
        { G2_PEEK() G2_RANGE(30, 32, -1) G2_NEED(4) }
        TS(11);
        G2_RANGE(32, NSTEP, -1)
        LDS_SPIN(ctl_min4(C_G2) >= 4);
        TS(6);
        {
            const f32x16 snd = kh ? c2a[0] : c2a[1];
            f32x16 keep = kh ? c2a[1] : c2a[0];
#pragma unroll
            for (int e = 0; e < 4; ++e) {
                f32x4 t = {snd[4 * e], snd[4 * e + 1], snd[4 * e + 2], snd[4 * e + 3]};
                lds_w128(L_EXCH + (u32)wave * 4096 + e * 1024 + lane * 16, __builtin_bit_cast(u32x4, t));
            }
            BARRIER();
            TS(7);
            const float omg = *(LAS float*)lp(L_CTL + C_OMG + (u32)(32 * sw + r32) * 4);
            const u32 gaa = L_CONV + (u32)sw * WBUF + GA_OFF + (u32)kh * 2048 + lane * 16;
            u32x4 ga0 = lds_r128(gaa), ga1 = lds_r128(gaa + 1024);
            u32 o[8];
#pragma unroll
            for (int e = 0; e < 4; ++e) {
                f32x4 t = __builtin_bit_cast(f32x4, lds_r128(L_EXCH + (u32)(wave ^ 2) * 4096 + e * 1024 + lane * 16));
                u32 gA = e < 2 ? ga0[2 * e] : ga1[2 * e - 4], gB = e < 2 ? ga0[2 * e + 1] : ga1[2 * e - 3];
                o[2 * e] = pk2(__builtin_fmaf(omg, keep[4 * e] + t[0], bflo(gA)), __builtin_fmaf(omg, keep[4 * e + 1] + t[1], bfhi(gA)));
                o[2 * e + 1] = pk2(__builtin_fmaf(omg, keep[4 * e + 2] + t[2], bflo(gB)), __builtin_fmaf(omg, keep[4 * e + 3] + t[3], bfhi(gB)));
            }
            GAS u32x4* op = (GAS u32x4*)(a.out + (size_t)(row2 + 32 * sw + r32) * D + c2 * 64 + 32 * kh + 16 * hh);
            op[0] = (u32x4){o[0], o[1], o[2], o[3]};
            op[1] = (u32x4){o[4], o[5], o[6], o[7]};
        }
        TS(8);
    } else {
        const int lw = wave - 4;
        const int ch = lw & 1;
        const int dch = xcd * 64 + 2 * l + ch;
        const u32 cb = L_CONV + (u32)ch * WBUF;
        const u32 wdv = WDV;
        u32x4 hc[4]; u32x2 hn[4];
        auto ssm_load = [&]() {
            const rsrc_t rh = mk_rsrc(a.ssmk + (size_t)dch * S, S * 2);
#pragma unroll
            for (int i = 0; i < 4; ++i) {
                hc[i] = __builtin_amdgcn_raw_buffer_load_b128(rh, (u32)(lane * 16 + i * 1024), 0, 0);
                hn[i] = __builtin_amdgcn_raw_buffer_load_b64(rh, (u32)(lane * 16 + i * 1024 + 16), 0, 0);
            }
        };
        if (lw < 2) {
            const u32 xs0 = (u32)(rt * 128 + lw * 64) * 1024;
            const u32 lx = L_X + lw * 8192;
            const u32 xo1 = xv1 + 7168, xo2 = xv0 + 2 * 7168, xo3 = xv1 + 3 * 7168, wo1 = wv[1] - 1024, wo2 = wv[2] - 2048;
            auto issue_x = [&](int s, int b) {
#pragma unroll
                for (int g = 0; g < 2; ++g) {
                    const u32 d = lx + b * 16384 + g * 4096, so = xs0 + g * 32768 + (s ^ rot) * 128;
                    DMA16O(rq, d, xv0, so, 0); DMA16O(rq, d, xo1, so, 1024); DMA16O(rq, d, xo2, so, 2048); DMA16O(rq, d, xo3, so, 3072);
                }
            };
            auto issue_w = [&](int s) {
#pragma unroll
                for (int g = 0; g < 4; ++g) {
                    const u32 d = lwb + (s & 3) * WBUF + g * 3072, so = ws0 + (u32)((s ^ rot) * 64 + g * 16) * (F * 2);
                    DMA16O(rw, d, wv[0], so, 0); DMA16O(rw, d, wo1, so, 1024); DMA16O(rw, d, wo2, so, 2048);
                }
            };
            BARRIER();
            __builtin_amdgcn_s_setprio(LDPRIO);
#define NEED_DONE(n) LDS_WAIT(ARRIVED(n))
            int b = 1;
#pragma unroll 1
            for (int n = -1; n < 6; ++n) {
                if (n > 0) NEED_DONE(n);
                issue_x(n + 2, b);
                issue_w(n + 2);
                if (lane == 0) __builtin_amdgcn_raw_ptr_buffer_load_lds(rq, (LAS void*)lp(L_CTL + C_LND + (n + 2) * 8 + lw * 4), 4, OOB, 0, 0, 0);
                b = b == 2 ? 0 : b + 1;
            }
            TS(1);
            const u32 wds = (u32)(lw * NSTEP * 16) * 1024u + (u32)c2 * 128;
            auto wd_run = [&](int j0, int n) {
#pragma unroll
                for (int j = 0; j < n; ++j) {
                    const u32 so = wds + (u32)KBLK(j0 + j) * 16384u;
                    DMA16O(rwd, slot_addr(j0) + lw * 2048 + j * 4096, wdv, so, 0);
                    DMA16O(rwd, slot_addr(j0) + lw * 2048 + j * 4096, wdv + 7168, so, 1024);
                }
            };
            wd_run(12, 3);
            ssm_load();
            NEED_DONE(6);
            wd_run(0, 4); wd_run(15, 6);
            TS(2);
            u32x4 t2[4], t3[4];
#pragma unroll
            for (int i = 0; i < 4; ++i) {
                t2[i] = (u32x4){smov(hc[i][1]), smov(hc[i][2]), smov(hc[i][3]), smov(hn[i][0])};
                t3[i] = (u32x4){__builtin_amdgcn_alignbit(hc[i][2], hc[i][1], 16), __builtin_amdgcn_alignbit(hc[i][3], hc[i][2], 16), __builtin_amdgcn_alignbit(hn[i][0], hc[i][3], 16), __builtin_amdgcn_alignbit(hn[i][1], hn[i][0], 16)};
                asm volatile("" : "+v"(t2[i]), "+v"(t3[i]));
            }
            const int sg = lane >> 1, half = lane & 1;
            const u32 d0 = rfl(hc[0][0]), a01 = __builtin_amdgcn_alignbit(rfl(hc[0][1]), d0, 16);
            u32x4 lead = {0u, 0u, (half && sg == 3) ? d0 << 16 : 0u, !half ? 0u : sg == 1 ? d0 << 16 : sg == 2 ? d0 : sg == 3 ? a01 : 0u};
            u32 leada = lane < 8 ? cb + sg * TOEP_CP + half * 16 : cb + U_OFF + (u32)(lane - 8) * 16;
            asm volatile("" : "+v"(lead), "+v"(leada));
            NEED_DONE(7);
            wd_run(4, 4);
            if (LDW1) wd_run(21, 6);
            if (lane == 0) __builtin_amdgcn_raw_ptr_buffer_load_lds(rq, (LAS void*)lp(L_CTL + C_LND + lw * 4), 4, OOB, 0, 0, 0);
            TS(9);
            TS(11);
            NEED_DONE(9);
            __builtin_amdgcn_s_setprio(HPW);
#pragma unroll
            for (int i = 0; i < 4; ++i) {
                lds_w128(cb + 32 + 2 * TOEP_CP + (u32)(lane * 16 + i * 1024), t2[i]);
                lds_w128(cb + 32 + 3 * TOEP_CP + (u32)(lane * 16 + i * 1024), t3[i]);
            }
            if (lane < 38) lds_w128(leada, lead);
            TS(3);
            __builtin_amdgcn_s_setprio(0);
        } else {
            __builtin_amdgcn_s_setprio(1);
            const u64 ep = __hip_atomic_load((const GAS u64*)(a.ws + WS_EPOCH), RLX_AGENT);
            const rsrc_t rk = mk_rsrc(a.k, S * D * 2), rv = mk_rsrc(a.v, S * D * 2);
            const int rblk = xcd * 8 + (l >> 3) * 2 + ch, R0 = rblk * 32, kgrp = l & 7, c4 = lane & 15, rg = lane >> 4;
            u32x2 kk[11], vv[8];
            u32x4 sw4[2];
#pragma unroll
            for (int i = 0; i < 2; ++i) sw4[i] = *(const GAS u32x4*)(a.sk + (kgrp * 64 + c4 * 4) * 4 + i * 8);
            BARRIER();
            if (KVD) __builtin_amdgcn_s_sleep(KVD);
            const u32 kvo = (u32)((R0 + 8 * rg) * 1024 + kgrp * 128 + c4 * 8);
#pragma unroll
            for (int i = 0; i < 3; ++i) kk[i] = __builtin_amdgcn_raw_buffer_load_b64(rk, R0 + 8 * rg > 0 ? kvo - (3 - i) * 1024 : OOB, 0, 0);
#pragma unroll
            for (int i = 0; i < 8; ++i) kk[3 + i] = __builtin_amdgcn_raw_buffer_load_b64(rk, kvo + i * 1024, 0, 0);
#pragma unroll
            for (int i = 0; i < 8; ++i) vv[i] = __builtin_amdgcn_raw_buffer_load_b64(rv, kvo + i * 1024, 0, 0);
            __builtin_amdgcn_s_setprio(0);
            if (ch == 0) {
                const u64 t = (((u64)rfl((u32)(ep >> 32)) << 32) | rfl((u32)ep)) + 0x9E3779B97F4A7C15ull;
                *(volatile LAS u32x2*)lp(L_CTL + C_TAG) = (u32x2){(u32)t, (u32)(t >> 32)};
                ctl_set(C_TAGOK, 1);
            }
            float tw[4][4];
#pragma unroll
            for (int c = 0; c < 4; ++c) {
                const u32 w01 = sw4[c >> 1][2 * (c & 1)], w23 = sw4[c >> 1][2 * (c & 1) + 1];
                tw[c][0] = bflo(w01); tw[c][1] = bfhi(w01); tw[c][2] = bflo(w23); tw[c][3] = bfhi(w23);
                asm volatile("" : "+v"(tw[c][0]), "+v"(tw[c][1]), "+v"(tw[c][2]), "+v"(tw[c][3]));
            }
            WAIT_VM(0);
            TS(10);
#pragma unroll
            for (int c = 0; c < 4; ++c) {
                float kf[11], e[8];
#pragma unroll
                for (int i = 0; i < 11; ++i) kf[i] = (c & 1) ? bfhi(kk[i][c >> 1]) : bflo(kk[i][c >> 1]);
#pragma unroll
                for (int i = 0; i < 8; ++i) e[i] = smul(kf[i], tw[c][3]);
#pragma unroll
                for (int t = 1; t < 4; ++t)
#pragma unroll
                    for (int i = 0; i < 8; ++i) e[i] = sfma(kf[i + t], tw[c][3 - t], e[i]);
#pragma unroll
                for (int i = 0; i < 8; ++i) e[i] = smul(e[i], (c & 1) ? bfhi(vv[i][c >> 1]) : bflo(vv[i][c >> 1]));
                const u32x4 o = {pk2(e[7], e[6]), pk2(e[5], e[4]), pk2(e[3], e[2]), pk2(e[1], e[0])};
                __builtin_amdgcn_raw_buffer_store_b128(o, rws, (u32)(c4 * 64 + rg * 16) + WS_KV + (u32)(rblk * 512 + c * 128 + kgrp * 16) * 64u, 0, SC1);
            }
            SB0();
            TS(11);
            LDS_WAIT(ctl_get(C_TAGOK) >= 1);
            const u64 tag = lds_tag();
            WAIT_VM(0);
            set_flag(a.ws, WS_F0 + (u32)(kgrp * 64 + rblk) * FL, tag, lane);
            const u32 wdt = __builtin_amdgcn_raw_buffer_load_b32(rwd, lane < 22 ? (u32)((bid * 2 + ch) * 22 + lane) * 128u : OOB, 0, 0);
            TS(9);
            const int grow = rt * 128 + ct * 8 + ch * 4;
            const u32x4 gwv = *(const GAS u32x4*)(a.gw + lane * 8);
            u32x4 qg[4];
#pragma unroll
            for (int i = 0; i < 4; ++i) qg[i] = __builtin_amdgcn_raw_buffer_load_b128(rq, (u32)lane * 16u, (u32)(grow + i) * 1024u, 0);
            {
                float z[4];
#pragma unroll
                for (int i = 0; i < 4; ++i) {
                    float s = smul(bflo(qg[i][0]), bflo(gwv[0]));
                    s = sfma(bfhi(qg[i][0]), bfhi(gwv[0]), s);
#pragma unroll
                    for (int c = 1; c < 4; ++c) { s = sfma(bflo(qg[i][c]), bflo(gwv[c]), s); s = sfma(bfhi(qg[i][c]), bfhi(gwv[c]), s); }
                    s = dpp_add<0x111>(s); s = dpp_add<0x112>(s); s = dpp_add<0x114>(s); s = dpp_add<0x118>(s);
                    s = dpp_add<0x142, 0xa>(s); s = dpp_add<0x143, 0xc>(s);
                    z[i] = __builtin_bit_cast(float, __builtin_amdgcn_readlane(__builtin_bit_cast(u32, s), 63));
                }
                const float z01 = (lane & 1) ? z[1] : z[0], z23 = (lane & 1) ? z[3] : z[2];
                const float zz = rbf((lane & 2) ? z23 : z01);
                const float g = rbf(__builtin_amdgcn_rcpf(1.f + __builtin_amdgcn_exp2f(-1.4426950408889634f * zz)));
                if (lane < 4) __builtin_amdgcn_raw_buffer_store_b32(__builtin_bit_cast(u32, g), rws, (u32)(grow + lane) * 4u, WS_G, SC1);
            }
            asm volatile("" ::"v"(wdt), "v"(cpf), "v"(tch0), "v"(tch1));
            LDS_WAIT(ARRIVED(HPAT));
            ssm_load();
            TS(1);
            poll_flags(a.ws, WS_F0 + (u32)(xcd * 64 + lane) * FL, tag);
            WAIT_VM(0);
            set_flag(a.ws, WS_F3 + (u32)(grow >> 2) * FL, tag, lane);
            u32x4 uu[4];
#pragma unroll
            for (int i = 0; i < 4; ++i) uu[i] = __builtin_amdgcn_raw_buffer_load_b128(rws, (u32)((lane >> 2) + 16 * i) * 32768u + (u32)(lane & 3) * 16u, WS_KV + (u32)((dch & 3) * 128 + (dch >> 2)) * 64u, SC1);
            u32x4 t1[4];
#pragma unroll
            for (int i = 0; i < 4; ++i) {
                t1[i] = (u32x4){__builtin_amdgcn_alignbit(hc[i][1], hc[i][0], 16), __builtin_amdgcn_alignbit(hc[i][2], hc[i][1], 16), __builtin_amdgcn_alignbit(hc[i][3], hc[i][2], 16), __builtin_amdgcn_alignbit(hn[i][0], hc[i][3], 16)};
                asm volatile("" : "+v"(t1[i]));
            }
            TS(2);
            LDS_WAIT(ARRIVED(9));
            __builtin_amdgcn_s_setprio(HPW);
#pragma unroll
            for (int i = 0; i < 4; ++i) {
                lds_w128(cb + 32 + (u32)(lane * 16 + i * 1024), hc[i]);
                lds_w128(cb + 32 + TOEP_CP + (u32)(lane * 16 + i * 1024), t1[i]);
                lds_w128(cb + U_OFF + 480 + lane * 16 + i * 1024, uu[i]);
            }
            TS(3);
        }
        ctl_set(C_CONVOK + lw * 4, 1);
        __builtin_amdgcn_s_setprio(0);
        u64 tag;
        const float Dv = bflo(a.ssmD[dch]);
        const int esw = lw & 1, edt = lw >> 1, r32 = lane & 31, hh = lane >> 5;
        const u32 qo = (u32)((row2 + 32 * esw + r32) * 1024 + c2 * 128 + edt * 64 + hh * 32);
        const u32x4 q0 = __builtin_amdgcn_raw_buffer_load_b128(rq, qo, 0, 0), q1 = __builtin_amdgcn_raw_buffer_load_b128(rq, qo + 16, 0, 0);
        {
            const int hf = lw >> 1;
            const int r = lane & 15, kg = lane >> 4;
            const int w0 = 9 + r - 8 * kg;
            const int sg = (w0 + 16) & 3;
            u32 aaddr = cb + (u32)sg * TOEP_CP + (u32)(w0 + 16 - sg) * 2 + hf * 256;
            u32 baddr = cb + U_OFF + 480 + (u32)(8 * kg + 16 * r - 16) * 2 - hf * 256 - 192;
            asm volatile("" : "+v"(aaddr), "+v"(baddr));
            f32x4 acc[8];
#pragma unroll
            for (int i = 0; i < 8; ++i) acc[i] = (f32x4){0.f, 0.f, 0.f, 0.f};
            bf16x8 A[2][8], B[2][8];
            auto ld = [&](int rho) {
#pragma unroll
                for (int pi = 0; pi < 8; ++pi) {
                    constexpr int LO[8] = {3, 4, 2, 1, 5, 0, 6, 7};
                    const int p = LO[pi];
                    u32x2 lo = *(volatile LAS u32x2*)lp(aaddr + rho * 64 + p * 512), hi = *(volatile LAS u32x2*)lp(aaddr + rho * 64 + p * 512 + 8);
                    A[rho & 1][p] = cat(lo, hi);
                    B[rho & 1][p] = asb(lds_r128(baddr + (3 - rho) * 64 + p * 512));
                }
            };
            auto mm = [&](int rho) {
                constexpr int OP[36] = {3, 3, 2, 2, 4, 2, 3, 1, 2, 4, 1, 1, 5, 1, 4, 3, 1, 5, 2, 1, 6, 0, 0, 0, 0, 0, 0, 0, 7, 6, 5, 4, 3, 2, 1, 0};
                constexpr int OQ[36] = {4, 3, 3, 2, 3, 4, 2, 3, 5, 2, 4, 2, 2, 5, 1, 1, 6, 1, 1, 1, 1, 6, 5, 4, 7, 3, 2, 1, 0, 0, 0, 0, 0, 0, 0, 0};
#pragma unroll
                for (int n = 0; n < 36; ++n) acc[OP[n] + OQ[n]] = MFMA16(A[rho & 1][OP[n]], B[rho & 1][OQ[n]], acc[OP[n] + OQ[n]]);
            };
            auto pat = [&]() {
#pragma unroll
                for (int i = 0; i < 12; ++i) { SGB(0x8, 1); SGB(0x100, 1); SGB(0x8, 1); SGB(0x100, 1); SGB(0x8, 1); }
            };
            LDS_SPIN(ctl_get(C_CONVOK + (lw ^ 2) * 4) >= 1);
            tag = lds_tag();
            ld(0);
            LDS_WAIT(ctl_get(C_GEG + lw * 4) >= 1);
            __builtin_amdgcn_s_setprio(CVPRIO);
            TS(4);
            WAIT_LGKM0();
            SB0();
            ld(1); mm(0); pat(); SB0();
            ld(2); mm(1); pat(); SB0();
            ld(3); mm(2); pat(); SB0();
            ctl_set(C_CDONE + lw * 4, 1);
            u32x2 uv[4];
            {
                const u32 uaddr = cb + U_OFF + 480 + (u32)(16 * r + 8 * (kg >> 1) + 4 * (1 - (kg & 1))) * 2 + hf * 2048;
#pragma unroll
                for (int i = 0; i < 4; ++i) uv[i] = *(volatile LAS u32x2*)lp(uaddr + i * 512);
            }
            SB0();
            mm(3); SB0();
            LDS_SPIN(ctl_get(C_CDONE + (lw ^ 2) * 4) >= 1);
#pragma unroll
            for (int i = 0; i < 4; ++i) lds_w128(cb + (u32)hf * 4096 + i * 1024 + lane * 16, __builtin_bit_cast(u32x4, hf ? acc[i] : acc[4 + i]));
            ctl_set(C_CDONE + lw * 4, 2);
            float kd[4][4];
#pragma unroll
            for (int i = 0; i < 4; ++i) { kd[i][0] = rbf(smul(bfhi(uv[i][1]), Dv)); kd[i][1] = rbf(smul(bflo(uv[i][1]), Dv)); kd[i][2] = rbf(smul(bfhi(uv[i][0]), Dv)); kd[i][3] = rbf(smul(bflo(uv[i][0]), Dv)); }
            LDS_SPIN(ctl_get(C_CDONE + (lw ^ 2) * 4) >= 2);
            f32x4 pp[4];
#pragma unroll
            for (int i = 0; i < 4; ++i) pp[i] = __builtin_bit_cast(f32x4, lds_r128(cb + (u32)(hf ^ 1) * 4096 + i * 1024 + lane * 16));
            u32x2 yo[4];
#pragma unroll
            for (int i = 0; i < 4; ++i) {
                const f32x4 m = hf ? acc[4 + i] : acc[i];
                const u32 r01 = pk2(sadd(m[0], pp[i][0]), sadd(m[1], pp[i][1])), r23 = pk2(sadd(m[2], pp[i][2]), sadd(m[3], pp[i][3]));
                yo[i] = (u32x2){pk2(sadd(bflo(r01), kd[i][0]), sadd(bfhi(r01), kd[i][1])), pk2(sadd(bflo(r23), kd[i][2]), sadd(bfhi(r23), kd[i][3]))};
            }
#pragma unroll
            for (int i = 0; i < 4; i += 2) {
                const auto s0 = __builtin_amdgcn_permlane16_swap(yo[i][0], yo[i + 1][0], false, false);
                const auto s1 = __builtin_amdgcn_permlane16_swap(yo[i][1], yo[i + 1][1], false, false);
                const u32x4 o = {s0[0], s1[0], s0[1], s1[1]};
                __builtin_amdgcn_raw_buffer_store_b128(o, rws, (u32)(32 * r + 16 * (kg >> 1) + (i + (kg & 1)) * 512) + WS_Y + (u32)dch * 4096u + (u32)hf * 2048u, 0, SC1);
            }
            TS(5);
            WAIT_VM(0);
            set_flag(a.ws, WS_F2 + (u32)(((xcd * 2 + hf) * 32 + l) * 2 + ch) * FL, tag, lane);
            TS(6);
        }
        LDS_WAIT(ctl_min4(C_G2) >= F2AT);
        TS(7);
        {
            const GAS u64* p = (const GAS u64*)(a.ws + (lane < 16 ? WS_F2 + (u32)((c2 * 2 + (row2 >> 10)) * 64 + lw * 16 + lane) * FL : WS_F3 + (u32)((row2 >> 2) + lw * 4 + (lane & 3)) * FL));
            bool ok = lane >= 20;
#pragma unroll 1
            for (u32 sp = 0; sp < SPIN_CAP; ++sp) {
                if (!ok) ok = __hip_atomic_load(p, RLX_AGENT) == tag;
                if (__all(ok)) break;
                __builtin_amdgcn_s_sleep(PSLEEP);
            }
            ctl_set(C_WDB + lw * 4, 1);
            LDS_SPIN(ctl_min4(C_WDB) >= 1);
        }
        const float gval = __builtin_bit_cast(float, __builtin_amdgcn_raw_buffer_load_b32(rws, (u32)(row2 + 32 * esw + r32) * 4u, WS_G, SC1));
        TS(8);
        const u32 yo = WS_Y + (u32)(c2 * 64 + edt * 32) * 4096u + (u32)(row2 + 32 * esw) * 2u;
        const u32x4 y0 = __builtin_amdgcn_raw_buffer_load_b128(rws, (u32)(lane >> 2) * 4096u + (u32)(lane & 3) * 16u, yo, SC1);
        const u32x4 y1 = __builtin_amdgcn_raw_buffer_load_b128(rws, (u32)((lane >> 2) + 16) * 4096u + (u32)(lane & 3) * 16u, yo, SC1);
        {
            const u32 ys = cb + YST_OFF + (u32)edt * 2048;
            lds_w128(ys + lane * 16, y0);
            lds_w128(ys + 1024 + lane * 16, y1);
            CFENCE();
            const int q4 = (lane & 15) >> 2, p4 = lane & 3, g1 = (lane >> 4) & 1;
            u32 go[8];
#pragma unroll
            for (int i = 0; i < 4; ++i) {
                u32x2 yv = lds_tr(ys + (u32)(16 * hh + 4 * i + q4) * 64 + g1 * 32 + p4 * 8);
                const u32 qa = i < 2 ? q0[2 * i] : q1[2 * i - 4], qb = i < 2 ? q0[2 * i + 1] : q1[2 * i - 3];
                const u32 pa = pk2(smul(bflo(qa), bflo(yv[0])), smul(bfhi(qa), bfhi(yv[0]))), pb = pk2(smul(bflo(qb), bflo(yv[1])), smul(bfhi(qb), bfhi(yv[1])));
                go[2 * i] = pk2(smul(gval, bflo(pa)), smul(gval, bfhi(pa)));
                go[2 * i + 1] = pk2(smul(gval, bflo(pb)), smul(gval, bfhi(pb)));
            }
            *(LAS float*)lp(L_CTL + C_OMG + (u32)(32 * esw + r32) * 4) = rbf(1.f - gval);
            lds_w128(cb + GA_OFF + (u32)edt * 2048 + lane * 16, (u32x4){go[0], go[1], go[2], go[3]});
            lds_w128(cb + GA_OFF + (u32)edt * 2048 + 1024 + lane * 16, (u32x4){go[4], go[5], go[6], go[7]});
        }
        if (bid == 0 && lw == 3) {
            const GAS u64* p = (const GAS u64*)(a.ws + WS_F0 + (u32)lane * 2 * FL);
#pragma unroll 1
            for (u32 sp = 0; sp < SPIN_CAP; ++sp) {
                u64 x0 = __hip_atomic_load(p, RLX_AGENT), x1 = __hip_atomic_load(p + 16 * FL, RLX_AGENT), x2 = __hip_atomic_load(p + 32 * FL, RLX_AGENT), x3 = __hip_atomic_load(p + 48 * FL, RLX_AGENT);
                if (sp == 0) BARRIER();
                if (__all(x0 == tag && x1 == tag && x2 == tag && x3 == tag)) break;
                __builtin_amdgcn_s_sleep(8);
            }
            set_flag(a.ws, WS_EPOCH, tag, lane);
        } else {
            BARRIER();
        }
    }
}

extern "C" void kernel_launch(void* const* d_in, const int* in_sizes, int n_in,
                              void* d_out, int out_size, void* d_ws, size_t ws_size,
                              hipStream_t stream) {
    Args a;
    a.q = (const u16*)d_in[0]; a.k = (const u16*)d_in[1]; a.v = (const u16*)d_in[2];
    a.sk = (const u16*)d_in[3]; a.ssmk = (const u16*)d_in[4]; a.ssmD = (const u16*)d_in[5];
    a.wg = (const u16*)d_in[7]; a.wu = (const u16*)d_in[8]; a.wd = (const u16*)d_in[9];
    a.gw = (const u16*)d_in[10];
    a.out = (u16*)d_out; a.ws = (char*)d_ws;
    fused_kernel<<<256, 512, 0, stream>>>(a);
}
```
